```python
import torch

def _softmax(x, axis=-1):
    return torch.softmax(x, dim=axis)
def _gelu(x):
    return torch.nn.functional.gelu(x, approximate="tanh")
def _silu(x):
    return torch.nn.functional.silu(x)
def _sigmoid(x):
    return torch.sigmoid(x)
def _layernorm(x, axis=-1, eps=1e-5):
    m = x.mean(dim=axis, keepdim=True)
    v = x.var(dim=axis, keepdim=True, unbiased=False)
    return (x - m) / torch.sqrt(v + eps)
def _scatter_add(tgt, idx, src, axis=0):
    return tgt.clone().index_add_(axis, idx, src)
def _segment_sum(src, seg, n):
    out = torch.zeros((n,) + src.shape[1:], dtype=src.dtype)
    return out.index_add_(0, seg, src)
def _pad(x, pw):
    flat = tuple(v for pair in reversed(pw) for v in pair)
    return torch.nn.functional.pad(x, flat)

kernel_name = 'parallel_sum__sliding_block_cached__glu_gated_glu__decode'

def setup_inputs():

    _w = (lambda a: (lambda t: t.to(torch.bfloat16) if t.is_floating_point() else t)(torch.as_tensor(a)))
    def _A_setup():
        B, Sq, D = 1, 1, 640
        Hq, Hkv, Sk, Dh = 32, 32, 32768, 128
        assert Hq % Hkv == 0, (Hq, Hkv)
        return {
            "x": _w((lambda *s: torch.randn(*s, dtype=torch.bfloat16) * 0.05)(B, Sq, D)),
            "k_cache": _w((lambda *s: torch.randn(*s, dtype=torch.bfloat16) * 3.2)(B, Hkv, Sk, Dh)),
            "v_cache": _w((lambda *s: torch.randn(*s, dtype=torch.bfloat16) * 1.6)(B, Hkv, Sk, Dh)),
            "wq": _w((lambda *s: torch.randn(*s, dtype=torch.bfloat16) * 3.2)(D, Hq * Dh) * (D ** -0.5)),
            "wo": _w((lambda *s: torch.randn(*s, dtype=torch.bfloat16) * 1.6)(Hq * Dh, D) * ((Hq * Dh) ** -0.5)),
        }
    _A_in = _A_setup()

    def _B_setup():
        B, S, D, F = 1, 1, 640, 1728
        return {
            "x": _w((lambda *s: torch.randn(*s, dtype=torch.bfloat16) * 0.05)(B, S, D)),
            "w_gate": _w((lambda *s: torch.randn(*s, dtype=torch.bfloat16) * 0.2)(D, F)),
            "w_up": _w((lambda *s: torch.randn(*s, dtype=torch.bfloat16) * 0.05)(D, F)),
            "w_down": _w((lambda *s: torch.randn(*s, dtype=torch.bfloat16) * 0.05)(F, D)),
        }
    _B_in = _B_setup()

    return {
        "A_x": _A_in["x"],
        "A_k_cache": _A_in["k_cache"],
        "A_v_cache": _A_in["v_cache"],
        "A_wq": _A_in["wq"],
        "A_wo": _A_in["wo"],
        "B_x": _B_in["x"],
        "B_w_gate": _B_in["w_gate"],
        "B_w_up": _B_in["w_up"],
        "B_w_down": _B_in["w_down"],
    }

def reference(A_x, A_k_cache, A_v_cache, A_wq, A_wo, B_x, B_w_gate, B_w_up, B_w_down):
    def _A_ref(x, k_cache, v_cache, wq, wo):
        B, Sq, D = x.shape
        Hq, Dh = 32, 128
        Hkv, Sk = k_cache.shape[1], k_cache.shape[2]
        W = min(4096, max(256, Sk // 4))
        rep = Hq // Hkv
        q = (lambda x, *d: x.permute(*d))((x @ wq).reshape(B, Sq, Hq, Dh), 0, 2, 1, 3)
        k = (lambda x, n, axis: torch.repeat_interleave(x, n, dim=axis))(k_cache[:, :, Sk - W:, :], rep, axis=1)
        v = (lambda x, n, axis: torch.repeat_interleave(x, n, dim=axis))(v_cache[:, :, Sk - W:, :], rep, axis=1)
        scores = torch.einsum("bhqd,bhkd->bhqk", q, k) * (Dh ** -0.5)
        p = _softmax(scores, axis=-1)
        o = torch.einsum("bhqk,bhkd->bhqd", p, v)
        return (lambda x, *d: x.permute(*d))(o, 0, 2, 1, 3).reshape(B, Sq, Hq * Dh) @ wo

    def _B_ref(x, w_gate, w_up, w_down):
        g = torch.einsum("bsd,df->bsf", x, w_gate)
        u = torch.einsum("bsd,df->bsf", x, w_up)
        h = _sigmoid(g) * u
        return torch.einsum("bsf,fd->bsd", h, w_down)

    x = A_x
    xn = _layernorm(x, axis=-1)
    return x + _A_ref(xn, A_k_cache, A_v_cache, A_wq, A_wo) + _B_ref(xn, B_w_gate, B_w_up, B_w_down)
```

```cpp
#include <hip/hip_runtime.h>
#include <hip/hip_bf16.h>
#include <cstdint>
#define MIMIC 0
typedef unsigned short u16;
typedef unsigned u32x4 __attribute__((ext_vector_type(4)));
constexpr int D = 640, HQ = 32, DH = 128, SK = 32768, W = 4096, F = 1728;
constexpr int NS = 8;
constexpr int KPS = W / NS;

__device__ __forceinline__ float bf2f(u16 b) { return __uint_as_float((unsigned)b << 16); }
__device__ __forceinline__ float rbf(float f) {
  return __bfloat162float(__float2bfloat16(f));
}
__device__ __forceinline__ float mrbf(float f) { return MIMIC ? rbf(f) : f; }

__device__ void ln_x(const u16* x, float* xn, float* red) {
  const int t = threadIdx.x;
  float v[3]; float s = 0.f;
  for (int i = 0; i < 3; ++i) { int k = t + 256 * i; v[i] = k < D ? bf2f(x[k]) : 0.f; s += v[i]; }
  for (int o = 32; o; o >>= 1) s += __shfl_xor(s, o);
  if ((t & 63) == 0) red[t >> 6] = s;
  __syncthreads();
  float mean = (red[0] + red[1] + red[2] + red[3]) / D;
  __syncthreads();
  float q = 0.f;
  for (int i = 0; i < 3; ++i) { int k = t + 256 * i; float d = k < D ? v[i] - mean : 0.f; q += d * d; }
  for (int o = 32; o; o >>= 1) q += __shfl_xor(q, o);
  if ((t & 63) == 0) red[t >> 6] = q;
  __syncthreads();
  float var = (red[0] + red[1] + red[2] + red[3]) / D;
  float m = mrbf(mean), vv = mrbf(var);
  float den = mrbf(sqrtf(mrbf(vv + 1e-5f)));
  for (int i = 0; i < 3; ++i) { int k = t + 256 * i; if (k < D) xn[k] = mrbf(mrbf(v[i] - m) / den); }
  __syncthreads();
}

__global__ void __launch_bounds__(256) k_proj(const u16* x, const u16* wq, const u16* wg, const u16* wu, float* q, float* g, float* u) {
  __shared__ float xn[D]; __shared__ float red[4]; __shared__ float part[32][64];
  ln_x(x, xn, red);
  int b = blockIdx.x; const u16* w; int ld; float* out; int c0;
  if (b < 64) { w = wq; ld = HQ * DH; out = q; c0 = b * 64; }
  else if (b < 64 + 27) { w = wg; ld = F; out = g; c0 = (b - 64) * 64; }
  else { w = wu; ld = F; out = u; c0 = (b - 91) * 64; }
  const int t = threadIdx.x, cl = t & 7, rg = t >> 3;
  float acc[8] = {0};
  for (int r = rg; r < D; r += 32) {
    u32x4 v = *(const u32x4*)(w + (size_t)r * ld + c0 + cl * 8);
    float xv = xn[r];
    for (int j = 0; j < 4; ++j) { acc[2 * j] += xv * __uint_as_float(v[j] << 16); acc[2 * j + 1] += xv * __uint_as_float(v[j] & 0xffff0000u); }
  }
  for (int j = 0; j < 8; ++j) part[rg][cl * 8 + j] = acc[j];
  __syncthreads();
  if (t < 64) { float s = 0.f; for (int r = 0; r < 32; ++r) s += part[r][t]; out[c0 + t] = mrbf(s); }
}

__global__ void __launch_bounds__(256) k_attn(const u16* kc, const u16* vc, const float* q, float* pm, float* pl, float* pacc) {
  __shared__ float sc[KPS]; __shared__ float red[8]; __shared__ float accs[16][DH];
  const int h = blockIdx.x / NS, s = blockIdx.x % NS, t = threadIdx.x, c = t & 15, r = t >> 4;
  const u16* kb = kc + ((size_t)h * SK + (SK - W) + s * KPS) * DH;
  const u16* vb = vc + ((size_t)h * SK + (SK - W) + s * KPS) * DH;
  float qv[8]; for (int j = 0; j < 8; ++j) qv[j] = q[h * DH + c * 8 + j];
  for (int k = r; k < KPS; k += 16) {
    u32x4 v = *(const u32x4*)(kb + (size_t)k * DH + c * 8);
    float d = 0.f;
    for (int j = 0; j < 4; ++j) { d += qv[2 * j] * __uint_as_float(v[j] << 16); d += qv[2 * j + 1] * __uint_as_float(v[j] & 0xffff0000u); }
    for (int o = 8; o; o >>= 1) d += __shfl_xor(d, o);
    if (c == 0) sc[k] = mrbf(mrbf(d) * 0.08838834764831845f);
  }
  __syncthreads();
  float m = fmaxf(sc[t], sc[t + 256]);
  for (int o = 32; o; o >>= 1) m = fmaxf(m, __shfl_xor(m, o));
  if ((t & 63) == 0) red[t >> 6] = m;
  __syncthreads();
  m = fmaxf(fmaxf(red[0], red[1]), fmaxf(red[2], red[3]));
  float p0 = __expf(sc[t] - m), p1 = __expf(sc[t + 256] - m);
  float l = p0 + p1;
  for (int o = 32; o; o >>= 1) l += __shfl_xor(l, o);
  if ((t & 63) == 0) red[4 + (t >> 6)] = l;
  __syncthreads();
  sc[t] = p0; sc[t + 256] = p1;
  l = red[4] + red[5] + red[6] + red[7];
  __syncthreads();
  float acc[8] = {0};
  for (int k = r; k < KPS; k += 16) {
    u32x4 v = *(const u32x4*)(vb + (size_t)k * DH + c * 8);
    float p = sc[k];
    for (int j = 0; j < 4; ++j) { acc[2 * j] += p * __uint_as_float(v[j] << 16); acc[2 * j + 1] += p * __uint_as_float(v[j] & 0xffff0000u); }
  }
  for (int j = 0; j < 8; ++j) accs[r][c * 8 + j] = acc[j];
  __syncthreads();
  if (t < DH) { float a = 0.f; for (int i = 0; i < 16; ++i) a += accs[i][t]; pacc[(size_t)blockIdx.x * DH + t] = a; }
  if (t == 0) { pm[blockIdx.x] = m; pl[blockIdx.x] = l; }
}

__global__ void __launch_bounds__(320) k_out(const u16* wo, const u16* wd, const float* pm, const float* pl, const float* pacc,
                                             const float* g, const float* u, float* part) {
  __shared__ float o[DH]; __shared__ float ps[4][D];
  const int b = blockIdx.x, t = threadIdx.x;
  int nrows; const u16* w;
  if (b < HQ) {
    nrows = DH; w = wo + (size_t)b * DH * D;
    if (t < DH) {
      float M = -INFINITY; for (int s = 0; s < NS; ++s) M = fmaxf(M, pm[b * NS + s]);
      float L = 0.f, a = 0.f;
      for (int s = 0; s < NS; ++s) { float e = __expf(pm[b * NS + s] - M); L += e * pl[b * NS + s]; a += e * pacc[(size_t)(b * NS + s) * DH + t]; }
      o[t] = mrbf(a / L);
    }
  } else {
    int c = b - HQ; nrows = 64; w = wd + (size_t)c * 64 * D;
    if (t < 64) { float gg = g[c * 64 + t], uu = u[c * 64 + t]; float sg = mrbf(1.f / (1.f + __expf(-gg))); o[t] = mrbf(sg * uu); }
  }
  __syncthreads();
  const int cl = t % 80, rg = t / 80;
  float acc[8] = {0};
  for (int r = rg; r < nrows; r += 4) {
    u32x4 v = *(const u32x4*)(w + (size_t)r * D + cl * 8);
    float p = o[r];
    for (int j = 0; j < 4; ++j) { acc[2 * j] += p * __uint_as_float(v[j] << 16); acc[2 * j + 1] += p * __uint_as_float(v[j] & 0xffff0000u); }
  }
  for (int j = 0; j < 8; ++j) ps[rg][cl * 8 + j] = acc[j];
  __syncthreads();
  for (int i = t; i < D; i += 320) part[(size_t)b * D + i] = ps[0][i] + ps[1][i] + ps[2][i] + ps[3][i];
}

__global__ void __launch_bounds__(64) k_fin(const u16* x, const float* part, __hip_bfloat16* out) {
  int i = blockIdx.x * 64 + threadIdx.x;
  float a = 0.f, bb = 0.f;
  for (int h = 0; h < HQ; ++h) a += part[(size_t)h * D + i];
  for (int c = 0; c < 27; ++c) bb += part[(size_t)(HQ + c) * D + i];
  float r = mrbf(bf2f(x[i]) + mrbf(a)) + mrbf(bb);
  out[i] = __float2bfloat16(r);
}

extern "C" void kernel_launch(void* const* d_in, const int* in_sizes, int n_in, void* d_out, int out_size, void* d_ws, size_t ws_size, hipStream_t stream) {
  const u16* x = (const u16*)d_in[0]; const u16* kc = (const u16*)d_in[1]; const u16* vc = (const u16*)d_in[2];
  const u16* wq = (const u16*)d_in[3]; const u16* wo = (const u16*)d_in[4];
  const u16* wg = (const u16*)d_in[6]; const u16* wu = (const u16*)d_in[7]; const u16* wd = (const u16*)d_in[8];
  float* ws = (float*)d_ws;
  float* q = ws; float* g = q + 4096; float* u = g + 2048; float* pm = u + 2048; float* pl = pm + 256; float* pacc = pl + 256; float* part = pacc + 256 * 128;
  k_proj<<<118, 256, 0, stream>>>(x, wq, wg, wu, q, g, u);
  k_attn<<<256, 256, 0, stream>>>(kc, vc, q, pm, pl, pacc);
  k_out<<<59, 320, 0, stream>>>(wo, wd, pm, pl, pacc, g, u, part);
  k_fin<<<10, 64, 0, stream>>>(x, part, (__hip_bfloat16*)d_out);
}
```

```cpp
#include <hip/hip_runtime.h>
#include <hip/hip_bf16.h>
#include <cstdint>

typedef unsigned short u16;
typedef unsigned u32x4 __attribute__((ext_vector_type(4)));
typedef float f32x4 __attribute__((ext_vector_type(4)));
typedef __bf16 bf16x2 __attribute__((ext_vector_type(2)));
typedef __attribute__((address_space(1))) unsigned long long gu64;
typedef __attribute__((address_space(1))) unsigned gu32;
typedef __attribute__((address_space(3))) unsigned lu32;
#define RLX_AGENT __ATOMIC_RELAXED, __HIP_MEMORY_SCOPE_AGENT
#define RLX_WG __ATOMIC_RELAXED, __HIP_MEMORY_SCOPE_WORKGROUP

constexpr int D = 640, HQ = 32, DH = 128, SK = 32768, WIN = 4096, F = 1728;
constexpr int NS = 8, KPS = WIN / NS;
constexpr int NW = 16;
constexpr int NGRP = F / 64;
constexpr int NFI = 14;
constexpr int NSRC = HQ + NFI;
constexpr int AUX_STREAM = 19;
constexpr int G_EPOCH = 0;
constexpr int G_QP = 64;
constexpr int G_GP = G_QP + HQ * NS * 128;
constexpr int G_AP = G_GP + NGRP * NS * 128;
constexpr int G_ML = G_AP + HQ * NS * 128;
constexpr int G_FP = G_ML + HQ * NS * 2;
constexpr int G_END = G_FP + 5 * NSRC * 128;
#define POLL1 4
#define POLL2 4
constexpr unsigned SPIN_G = 1u << 18, SPIN_L = 1u << 22;

#define STAMP(i) do {} while (0)

__device__ __forceinline__ float lo_f(unsigned v) { return __uint_as_float(v << 16); }
__device__ __forceinline__ float hi_f(unsigned v) { return __uint_as_float(v & 0xffff0000u); }
__device__ __forceinline__ float rbf(float f) { return (float)(__bf16)f; }
__device__ __forceinline__ unsigned bf_bits(float f) { return (unsigned)__builtin_bit_cast(unsigned short, (__bf16)f); }
__device__ __forceinline__ float dot2(unsigned a, unsigned b, float acc) {
  return __builtin_amdgcn_fdot2_f32_bf16(__builtin_bit_cast(bf16x2, a), __builtin_bit_cast(bf16x2, b), acc, false);
}
template <int CTRL> __device__ __forceinline__ float dpp(float x) {
  return __builtin_bit_cast(float, __builtin_amdgcn_mov_dpp(__builtin_bit_cast(int, x), CTRL, 0xf, 0xf, true));
}
constexpr int XOR1 = 0xB1, XOR2 = 0x4E, HMIR = 0x141, ROR8 = 0x128;
__device__ __forceinline__ float x16_sum(float x) { auto s = __builtin_amdgcn_permlane16_swap(__float_as_uint(x), __float_as_uint(x), false, false); return __uint_as_float(s[0]) + __uint_as_float(s[1]); }
__device__ __forceinline__ float x32_sum(float x) { auto s = __builtin_amdgcn_permlane32_swap(__float_as_uint(x), __float_as_uint(x), false, false); return __uint_as_float(s[0]) + __uint_as_float(s[1]); }
__device__ __forceinline__ float x16_max(float x) { auto s = __builtin_amdgcn_permlane16_swap(__float_as_uint(x), __float_as_uint(x), false, false); return fmaxf(__uint_as_float(s[0]), __uint_as_float(s[1])); }
__device__ __forceinline__ float x32_max(float x) { auto s = __builtin_amdgcn_permlane32_swap(__float_as_uint(x), __float_as_uint(x), false, false); return fmaxf(__uint_as_float(s[0]), __uint_as_float(s[1])); }
__device__ __forceinline__ float sum8(float x) { x += dpp<XOR1>(x); x += dpp<XOR2>(x); x += dpp<HMIR>(x); return x; }
__device__ __forceinline__ float max8(float x) { x = fmaxf(x, dpp<XOR1>(x)); x = fmaxf(x, dpp<XOR2>(x)); x = fmaxf(x, dpp<HMIR>(x)); return x; }
__device__ __forceinline__ float sum16(float x) { x = sum8(x); x += dpp<ROR8>(x); return x; }
__device__ __forceinline__ float sum64(float x) { return x32_sum(x16_sum(sum16(x))); }
__device__ __forceinline__ void fma8(float (&a)[8], u32x4 w, float f) {
#pragma unroll
  for (int k = 0; k < 4; ++k) { a[2 * k] = fmaf(f, lo_f(w[k]), a[2 * k]); a[2 * k + 1] = fmaf(f, hi_f(w[k]), a[2 * k + 1]); }
}
__device__ __forceinline__ __amdgpu_buffer_rsrc_t mkrs(const void* p, unsigned bytes) { return __builtin_amdgcn_make_buffer_rsrc((void*)p, 0, bytes, 0x00020000); }
__device__ __forceinline__ u32x4 ldb(__amdgpu_buffer_rsrc_t rs, int voff, int soff) { return __builtin_amdgcn_raw_buffer_load_b128(rs, voff, soff, AUX_STREAM); }
__device__ __forceinline__ void put(gu64* g, unsigned tag, float v) { __hip_atomic_store(g, ((unsigned long long)tag << 32) | __float_as_uint(v), RLX_AGENT); }
__device__ __forceinline__ unsigned long long get(gu64* g) { return __hip_atomic_load(g, RLX_AGENT); }
#define LGKM0() asm volatile("s_waitcnt lgkmcnt(0)" ::: "memory")
#define SB() __builtin_amdgcn_sched_barrier(0)

struct Lds {
  float red[NW][128];
  float red3[NW][128];
  float p1[8][128];
  float m[NW], l[NW];
  unsigned qs[64];
  unsigned cnt[4];
  unsigned tagw[4];
};

__global__ void __launch_bounds__(1024) fused_kernel(const u16* __restrict__ x, const u16* __restrict__ kc, const u16* __restrict__ vc,
                                                     const u16* __restrict__ wq, const u16* __restrict__ wo, const u16* __restrict__ wg,
                                                     const u16* __restrict__ wu, const u16* __restrict__ wd, u16* __restrict__ out,
                                                     unsigned long long* __restrict__ wsp) {
  __shared__ __attribute__((aligned(16))) Lds L;
  gu64* ws = (gu64*)wsp;
  const int b = blockIdx.x, h = b >> 3, s = (b + h) & 7;
  const int tid = threadIdx.x, lane = tid & 63, w = __builtin_amdgcn_readfirstlane(tid >> 6);
  const int r = lane >> 4, c = lane & 15;
  STAMP(0);
  if (tid < 4) L.cnt[tid] = 0u;
  const bool qwave = w < 4, gwave = (w >= 4) && (w < 8) && (h < NGRP);
  const bool p1wave = qwave || gwave;
  if (!p1wave) {
    LGKM0();
    __builtin_amdgcn_s_barrier();
  } else {
    __builtin_amdgcn_s_setprio(3);
    const __amdgpu_buffer_rsrc_t rsx = mkrs(x, D * 2);
    unsigned xr[5]; u32x4 wv[5], xa, xb;
    const int row0 = 80 * s + (qwave ? 20 * w + r : 40 * (w & 1) + (lane >> 3));
    const int rstep = qwave ? 4 : 8, rbytes = qwave ? HQ * DH * 2 : F * 2;
    const int coff = qwave ? h * 256 + c * 16 : h * 128 + (lane & 7) * 16;
    const __amdgpu_buffer_rsrc_t rsw = mkrs(qwave ? wq : (w < 6 ? wg : wu), qwave ? D * HQ * DH * 2 : D * F * 2);
    xa = __builtin_amdgcn_raw_buffer_load_b128(rsx, lane * 16, 0, 0);
    xb = __builtin_amdgcn_raw_buffer_load_b128(rsx, 1024 + lane * 16, 0, 0);
#pragma unroll
    for (int i = 0; i < 5; ++i) wv[i] = ldb(rsw, row0 * rbytes + coff, i * rstep * rbytes);
#pragma unroll
    for (int i = 0; i < 5; ++i) xr[i] = __builtin_amdgcn_raw_buffer_load_b16(rsx, row0 * 2, i * rstep * 2, 0);
    const unsigned ep = __builtin_amdgcn_raw_buffer_load_b32(mkrs(wsp + G_EPOCH, (w & 3) == 0 ? 4 : 0), 0, 0, 16);
    SB();
    LGKM0();
    __builtin_amdgcn_s_barrier();
    SB();
    float sm = 0.f;
#pragma unroll
    for (int k = 0; k < 4; ++k) sm += (lo_f(xa[k]) + hi_f(xa[k])) + (lo_f(xb[k]) + hi_f(xb[k]));
    const float mean = sum64(sm) * (1.0f / D);
    float sq = 0.f, sq2 = 0.f;
#pragma unroll
    for (int k = 0; k < 4; ++k) {
      float d0 = lo_f(xa[k]) - mean, d1 = hi_f(xa[k]) - mean, d2 = lo_f(xb[k]) - mean, d3 = hi_f(xb[k]) - mean;
      sq += d0 * d0 + d1 * d1; sq2 += d2 * d2 + d3 * d3;
    }
    sq += lane < 16 ? sq2 : 0.f;
    const float var = sum64(sq) * (1.0f / D);
    const float mb = rbf(mean), den = rbf(sqrtf(rbf(rbf(var) + 1e-5f)));
    float acc[8];
#pragma unroll
    for (int k = 0; k < 8; ++k) acc[k] = 0.f;
#pragma unroll
    for (int i = 0; i < 5; ++i) { const float xn = rbf(rbf(lo_f(xr[i]) - mb) / den); fma8(acc, wv[i], xn); }
    if (qwave) {
#pragma unroll
      for (int k = 0; k < 8; ++k) acc[k] = x32_sum(x16_sum(acc[k]));
      if (lane < 16) { *(f32x4*)&L.p1[w][8 * c] = f32x4{acc[0], acc[1], acc[2], acc[3]}; *(f32x4*)&L.p1[w][8 * c + 4] = f32x4{acc[4], acc[5], acc[6], acc[7]}; }
    } else {
#pragma unroll
      for (int k = 0; k < 8; ++k) { float t = acc[k]; t += dpp<ROR8>(t); acc[k] = x32_sum(x16_sum(t)); }
      if (lane < 8) { *(f32x4*)&L.p1[w][8 * lane] = f32x4{acc[0], acc[1], acc[2], acc[3]}; *(f32x4*)&L.p1[w][8 * lane + 4] = f32x4{acc[4], acc[5], acc[6], acc[7]}; }
    }
    if ((w & 3) == 0 && lane == 0) L.tagw[w >> 2] = ep + 1u;
    LGKM0();
    STAMP(1);
    unsigned old = 0;
    if (lane == 0) old = __hip_atomic_fetch_add(&L.cnt[qwave ? 0 : 1], 1u, RLX_WG);
    old = __builtin_amdgcn_readfirstlane(old);
    asm volatile("" ::: "memory");
    if (old == 3) {
      const unsigned tag = L.tagw[w >> 2];
      if (qwave) {
        const float v0 = (L.p1[0][lane] + L.p1[1][lane]) + (L.p1[2][lane] + L.p1[3][lane]);
        const float v1 = (L.p1[0][lane + 64] + L.p1[1][lane + 64]) + (L.p1[2][lane + 64] + L.p1[3][lane + 64]);
        gu64* g = ws + G_QP + (h * NS + s) * 128;
        put(g + lane, tag, v0); put(g + 64 + lane, tag, v1);
        STAMP(2);
      } else {
        const float v0 = L.p1[4][lane] + L.p1[5][lane], v1 = L.p1[6][lane] + L.p1[7][lane];
        gu64* g = ws + G_GP + (h * NS + s) * 128;
        put(g + lane, tag, v0); put(g + 64 + lane, tag, v1);
      }
    }
    if (!qwave) __builtin_amdgcn_s_setprio(0);
  }

  const size_t kvoff = ((size_t)h * SK + (SK - WIN) + s * KPS + w * 32) * DH;
  const __amdgpu_buffer_rsrc_t rsk = mkrs(kc + kvoff, 32 * DH * 2), rsv = mkrs(vc + kvoff, 32 * DH * 2);
  u32x4 kr[8], vr[8], w3[2];
#pragma unroll
  for (int i = 0; i < 8; ++i) kr[i] = ldb(rsk, lane * 16, i * 1024);
  SB();
#pragma unroll
  for (int i = 0; i < 8; ++i) vr[i] = ldb(rsv, lane * 16, i * 1024);
  SB();
  const int fj = (HQ - 1 - h) * 3 + (s - 5);
  const bool typeA = s < 5, typeB = !typeA && fj < NFI * 5;
  const int fgi = fj / 5, slice = typeA ? s : fj % 5;
  {
    const u16* wb = typeA ? wo + (size_t)h * DH * D : wd + (size_t)(typeB ? fgi : 0) * 128 * D;
    const unsigned nb = typeA ? DH * D * 2 : (typeB ? (fgi == NFI - 1 ? 64 : 128) * D * 2 : 0);
    const __amdgpu_buffer_rsrc_t rs3 = mkrs(wb, nb);
#pragma unroll
    for (int i = 0; i < 2; ++i) w3[i] = ldb(rs3, (8 * w + 4 * i + r) * (D * 2) + slice * 256 + c * 16, 0);
  }
  SB();
  STAMP(4);
  if (qwave) {
    for (unsigned spin = 0; spin < SPIN_L; ++spin) { if (__hip_atomic_load(&L.cnt[0], RLX_WG) >= 4u) break; __builtin_amdgcn_s_sleep(1); }
    asm volatile("" ::: "memory");
    const unsigned tag = L.tagw[0];
    gu64* g = ws + G_QP + (h * NS + (lane >> 5)) * 128 + 32 * w + (lane & 31);
    unsigned long long t[4];
#pragma unroll
    for (int i = 0; i < 4; ++i) t[i] = get(g + i * 256);
    for (unsigned spin = 0; spin < SPIN_G; ++spin) {
      bool ok = true;
#pragma unroll
      for (int i = 0; i < 4; ++i) ok &= (unsigned)(t[i] >> 32) == tag;
      if (__all(ok)) break;
      __builtin_amdgcn_s_sleep(8);
#pragma unroll
      for (int i = 0; i < 4; ++i) if ((unsigned)(t[i] >> 32) != tag) t[i] = get(g + i * 256);
    }
    const float qv = x32_sum((__uint_as_float((unsigned)t[0]) + __uint_as_float((unsigned)t[1])) + (__uint_as_float((unsigned)t[2]) + __uint_as_float((unsigned)t[3])));
    if (lane < 32) ((u16*)L.qs)[32 * w + lane] = (u16)bf_bits(qv);
    LGKM0();
    if (lane == 0) __hip_atomic_fetch_add(&L.cnt[2], 1u, RLX_WG);
    __builtin_amdgcn_s_setprio(0);
    STAMP(3);
  }
  for (unsigned spin = 0; spin < SPIN_L; ++spin) {
    if (__hip_atomic_load(&L.cnt[2], RLX_WG) >= 4u) break;
    __builtin_amdgcn_s_sleep(1);
  }
  asm volatile("" ::: "memory");
  const u32x4 qp = *(const u32x4*)&L.qs[4 * c];
  const unsigned tag = __builtin_amdgcn_readfirstlane(L.tagw[0]);
  STAMP(5);
  float sc[8];
#pragma unroll
  for (int i = 0; i < 8; ++i) {
    float d = dot2(kr[i][0], qp[0], 0.f);
#pragma unroll
    for (int k = 1; k < 4; ++k) d = dot2(kr[i][k], qp[k], d);
    d = sum16(d);
    sc[i] = rbf(rbf(d) * 0.08838834764831845f);
  }
  float mx = sc[0];
#pragma unroll
  for (int i = 1; i < 8; ++i) mx = fmaxf(mx, sc[i]);
  mx = x32_max(x16_max(mx));
  constexpr float LOG2E = 1.4426950408889634f;
  float ls = 0.f;
#pragma unroll
  for (int i = 0; i < 8; ++i) { sc[i] = __builtin_amdgcn_exp2f((sc[i] - mx) * LOG2E); ls += sc[i]; }
  ls = x32_sum(x16_sum(ls));
  STAMP(6);
  float acc[8];
#pragma unroll
  for (int k = 0; k < 8; ++k) acc[k] = 0.f;
#pragma unroll
  for (int i = 0; i < 8; ++i) fma8(acc, vr[i], sc[i]);
#pragma unroll
  for (int k = 0; k < 8; ++k) acc[k] = x32_sum(x16_sum(acc[k]));
  if (lane < 16) {
    *(f32x4*)&L.red[w][8 * c] = f32x4{acc[0], acc[1], acc[2], acc[3]}; *(f32x4*)&L.red[w][8 * c + 4] = f32x4{acc[4], acc[5], acc[6], acc[7]};
    if (lane == 0) { L.m[w] = mx; L.l[w] = ls; }
  }
  STAMP(7);
  LGKM0();
  __builtin_amdgcn_s_barrier();
  asm volatile("" ::: "memory");
  if (w < 2) {
    float mw[NW], lw[NW];
#pragma unroll
    for (int i = 0; i < NW; i += 4) { const f32x4 a = *(const f32x4*)&L.m[i], bb = *(const f32x4*)&L.l[i];
#pragma unroll
      for (int k = 0; k < 4; ++k) { mw[i + k] = a[k]; lw[i + k] = bb[k]; } }
    float M = mw[0];
#pragma unroll
    for (int i = 1; i < NW; ++i) M = fmaxf(M, mw[i]);
    float a = 0.f, Ls = 0.f;
#pragma unroll
    for (int i = 0; i < NW; ++i) { const float e = __builtin_amdgcn_exp2f((mw[i] - M) * LOG2E); a = fmaf(e, L.red[i][tid], a); Ls = fmaf(e, lw[i], Ls); }
    put(ws + G_AP + (h * NS + s) * 128 + tid, tag, a);
    if (tid < 2) put(ws + G_ML + (h * NS + s) * 2 + tid, tag, tid == 0 ? M : Ls);
  }
  STAMP(8);
  if (typeA || typeB) {
    const int p = lane & 7, j = 4 * ((lane >> 3) & 1) + r;
    float own;
    if (typeA) {
      gu64* ga = ws + G_AP + (h * NS + p) * 128 + 8 * w + j;
      const __amdgpu_buffer_rsrc_t rsm = mkrs(wsp + G_ML + h * NS * 2, NS * 16);
      unsigned long long ta = get(ga);
      u32x4 ml = __builtin_amdgcn_raw_buffer_load_b128(rsm, p * 16, 0, 16);
      for (unsigned spin = 0; spin < SPIN_G; ++spin) {
        const bool ok = (unsigned)(ta >> 32) == tag && ml[1] == tag && ml[3] == tag;
        if (__all(ok)) break;
        __builtin_amdgcn_s_sleep(POLL1);
        asm volatile("" ::: "memory");
        if (!ok) { ta = get(ga); ml = __builtin_amdgcn_raw_buffer_load_b128(rsm, p * 16, 0, 16); }
      }
      const unsigned tm = ml[0], tl = ml[2];
      const float mp = __uint_as_float((unsigned)tm), M = max8(mp);
      const float e = __builtin_amdgcn_exp2f((mp - M) * LOG2E);
      const float Ls = sum8(e * __uint_as_float((unsigned)tl)), a = sum8(e * __uint_as_float((unsigned)ta));
      own = rbf(a / Ls);
    } else {
      const int rr = 8 * w + j, grp = min(2 * fgi + (rr >> 6), NGRP - 1);
      gu64* gg = ws + G_GP + (grp * NS + p) * 128 + (rr & 63);
      unsigned long long tg = get(gg), tu = get(gg + 64);
      for (unsigned spin = 0; spin < SPIN_G; ++spin) {
        const bool ok = (unsigned)(tg >> 32) == tag && (unsigned)(tu >> 32) == tag;
        if (__all(ok)) break;
        __builtin_amdgcn_s_sleep(8);
        if (!ok) { tg = get(gg); tu = get(gg + 64); }
      }
      const float g = rbf(sum8(__uint_as_float((unsigned)tg))), u = rbf(sum8(__uint_as_float((unsigned)tu)));
      const float sg = rbf(1.0f / (1.0f + __expf(-g)));
      own = rbf(sg * u);
    }
    STAMP(9);
    const float oth = dpp<ROR8>(own);
    const bool hi = (lane >> 3) & 1;
    const float o0 = hi ? oth : own, o1 = hi ? own : oth;
    float a3[8];
#pragma unroll
    for (int k = 0; k < 8; ++k) a3[k] = 0.f;
    fma8(a3, w3[0], o0); fma8(a3, w3[1], o1);
#pragma unroll
    for (int k = 0; k < 8; ++k) a3[k] = x32_sum(x16_sum(a3[k]));
    if (lane < 16) { *(f32x4*)&L.red3[w][8 * c] = f32x4{a3[0], a3[1], a3[2], a3[3]}; *(f32x4*)&L.red3[w][8 * c + 4] = f32x4{a3[4], a3[5], a3[6], a3[7]}; }
    LGKM0();
    __builtin_amdgcn_s_barrier();
    asm volatile("" ::: "memory");
    if (w < 2) {
      float a = 0.f;
#pragma unroll
      for (int i = 0; i < NW; ++i) a += L.red3[i][tid];
      put(ws + G_FP + (slice * NSRC + (typeA ? h : HQ + fgi)) * 128 + tid, tag, a);
    }
    STAMP(10);
  }
  const int rj = h * 3 + (s - 5);
  if (!typeA && rj < 80 && w == 2) {
    const int p = lane >> 3, cc = lane & 7, col = 8 * rj + cc;
    gu64* g = ws + G_FP + ((rj >> 4) * NSRC + p) * 128 + 8 * (rj & 15) + cc;
    const float xv = lo_f(x[col]);
    unsigned long long t[6];
#pragma unroll
    for (int i = 0; i < 6; ++i) t[i] = (8 * i + p < NSRC) ? get(g + i * 8 * 128) : ((unsigned long long)tag << 32);
    for (unsigned spin = 0; spin < SPIN_G; ++spin) {
      bool ok = true;
#pragma unroll
      for (int i = 0; i < 6; ++i) ok &= (unsigned)(t[i] >> 32) == tag;
      if (__all(ok)) break;
      __builtin_amdgcn_s_sleep(POLL2);
#pragma unroll
      for (int i = 0; i < 6; ++i) if ((unsigned)(t[i] >> 32) != tag) t[i] = get(g + i * 8 * 128);
    }
    float v[6];
#pragma unroll
    for (int i = 0; i < 6; ++i) v[i] = __uint_as_float((unsigned)t[i]);
    float a = (v[0] + v[1]) + (v[2] + v[3]), bb = v[4] + v[5];
    a += dpp<ROR8>(a); a = x32_sum(x16_sum(a));
    bb += dpp<ROR8>(bb); bb = x32_sum(x16_sum(bb));
    const float res = rbf(xv + rbf(a)) + rbf(bb);
    if (lane < 8) out[col] = (u16)bf_bits(res);
    if (rj == 0 && lane == 0) __hip_atomic_store((gu32*)(ws + G_EPOCH), tag, RLX_AGENT);
    STAMP(11);
  }
}

extern "C" void kernel_launch(void* const* d_in, const int* in_sizes, int n_in, void* d_out, int out_size, void* d_ws, size_t ws_size, hipStream_t stream) {
  fused_kernel<<<256, 1024, 0, stream>>>((const u16*)d_in[0], (const u16*)d_in[1], (const u16*)d_in[2], (const u16*)d_in[3], (const u16*)d_in[4],
                                         (const u16*)d_in[6], (const u16*)d_in[7], (const u16*)d_in[8], (u16*)d_out, (unsigned long long*)d_ws);
}
```

```cpp
#include <hip/hip_runtime.h>
#include <hip/hip_bf16.h>
#include <cstdint>

extern "C" __device__ unsigned long long dispatch_id_intrin() __asm("llvm.amdgcn.dispatch.id");
typedef unsigned short u16;
typedef unsigned u32x4 __attribute__((ext_vector_type(4)));
typedef float f32x4 __attribute__((ext_vector_type(4)));
typedef __bf16 bf16x2 __attribute__((ext_vector_type(2)));
typedef __attribute__((address_space(1))) unsigned long long gu64;
typedef __attribute__((address_space(1))) unsigned gu32;
typedef __attribute__((address_space(3))) unsigned lu32;
#define RLX_AGENT __ATOMIC_RELAXED, __HIP_MEMORY_SCOPE_AGENT
#define RLX_WG __ATOMIC_RELAXED, __HIP_MEMORY_SCOPE_WORKGROUP

constexpr int D = 640, HQ = 32, DH = 128, SK = 32768, WIN = 4096, F = 1728;
constexpr int NS = 8, KPS = WIN / NS;
constexpr int NW = 16;
constexpr int NGRP = F / 64;
constexpr int NFI = 14;
constexpr int NSRC = HQ + NFI;
constexpr int AUX_STREAM = 19;
constexpr int G_EPOCH = 0;
constexpr int G_QP = 64;
constexpr int G_GP = G_QP + HQ * NS * 128;
constexpr int G_AP = G_GP + NGRP * NS * 128;
constexpr int G_ML = G_AP + HQ * NS * 128;
constexpr int G_FP = G_ML + HQ * NS * 2;
constexpr int G_END = G_FP + 5 * NSRC * 128;
#define POLL1 4
#define POLL2 4
constexpr unsigned SPIN_G = 1u << 18, SPIN_L = 1u << 22;

#define STAMP(i) do {} while (0)

__device__ __forceinline__ float lo_f(unsigned v) { return __uint_as_float(v << 16); }
__device__ __forceinline__ float hi_f(unsigned v) { return __uint_as_float(v & 0xffff0000u); }
__device__ __forceinline__ float rbf(float f) { return (float)(__bf16)f; }
__device__ __forceinline__ unsigned bf_bits(float f) { return (unsigned)__builtin_bit_cast(unsigned short, (__bf16)f); }
__device__ __forceinline__ float dot2(unsigned a, unsigned b, float acc) {
  return __builtin_amdgcn_fdot2_f32_bf16(__builtin_bit_cast(bf16x2, a), __builtin_bit_cast(bf16x2, b), acc, false);
}
template <int CTRL> __device__ __forceinline__ float dpp(float x) {
  return __builtin_bit_cast(float, __builtin_amdgcn_mov_dpp(__builtin_bit_cast(int, x), CTRL, 0xf, 0xf, true));
}
constexpr int XOR1 = 0xB1, XOR2 = 0x4E, HMIR = 0x141, ROR8 = 0x128;
__device__ __forceinline__ float x16_sum(float x) { auto s = __builtin_amdgcn_permlane16_swap(__float_as_uint(x), __float_as_uint(x), false, false); return __uint_as_float(s[0]) + __uint_as_float(s[1]); }
__device__ __forceinline__ float x32_sum(float x) { auto s = __builtin_amdgcn_permlane32_swap(__float_as_uint(x), __float_as_uint(x), false, false); return __uint_as_float(s[0]) + __uint_as_float(s[1]); }
__device__ __forceinline__ float x16_max(float x) { auto s = __builtin_amdgcn_permlane16_swap(__float_as_uint(x), __float_as_uint(x), false, false); return fmaxf(__uint_as_float(s[0]), __uint_as_float(s[1])); }
__device__ __forceinline__ float x32_max(float x) { auto s = __builtin_amdgcn_permlane32_swap(__float_as_uint(x), __float_as_uint(x), false, false); return fmaxf(__uint_as_float(s[0]), __uint_as_float(s[1])); }
__device__ __forceinline__ float sum8(float x) { x += dpp<XOR1>(x); x += dpp<XOR2>(x); x += dpp<HMIR>(x); return x; }
__device__ __forceinline__ float max8(float x) { x = fmaxf(x, dpp<XOR1>(x)); x = fmaxf(x, dpp<XOR2>(x)); x = fmaxf(x, dpp<HMIR>(x)); return x; }
__device__ __forceinline__ float sum16(float x) { x = sum8(x); x += dpp<ROR8>(x); return x; }
__device__ __forceinline__ float sum64(float x) { return x32_sum(x16_sum(sum16(x))); }
__device__ __forceinline__ void fma8(float (&a)[8], u32x4 w, float f) {
#pragma unroll
  for (int k = 0; k < 4; ++k) { a[2 * k] = fmaf(f, lo_f(w[k]), a[2 * k]); a[2 * k + 1] = fmaf(f, hi_f(w[k]), a[2 * k + 1]); }
}
__device__ __forceinline__ __amdgpu_buffer_rsrc_t mkrs(const void* p, unsigned bytes) { return __builtin_amdgcn_make_buffer_rsrc((void*)p, 0, bytes, 0x00020000); }
__device__ __forceinline__ u32x4 ldb(__amdgpu_buffer_rsrc_t rs, int voff, int soff) { return __builtin_amdgcn_raw_buffer_load_b128(rs, voff, soff, AUX_STREAM); }
__device__ __forceinline__ void put(gu64* g, unsigned tag, float v) { __hip_atomic_store(g, ((unsigned long long)tag << 32) | __float_as_uint(v), RLX_AGENT); }
__device__ __forceinline__ unsigned long long get(gu64* g) { return __hip_atomic_load(g, RLX_AGENT); }
#define LGKM0() asm volatile("s_waitcnt lgkmcnt(0)" ::: "memory")
#define SB() __builtin_amdgcn_sched_barrier(0)

struct Lds {
  float red[NW][128];
  float red3[NW][128];
  float p1[8][128];
  float m[NW], l[NW];
  unsigned qs[64];
  unsigned cnt[4];
  unsigned tagw[4];
};

__global__ void __launch_bounds__(1024) fused_kernel(const u16* __restrict__ x, const u16* __restrict__ kc, const u16* __restrict__ vc,
                                                     const u16* __restrict__ wq, const u16* __restrict__ wo, const u16* __restrict__ wg,
                                                     const u16* __restrict__ wu, const u16* __restrict__ wd, u16* __restrict__ out,
                                                     unsigned long long* __restrict__ wsp) {
  __shared__ __attribute__((aligned(16))) Lds L;
  gu64* ws = (gu64*)wsp;
  const unsigned long long tstart = __builtin_amdgcn_s_memrealtime();
  const int b = blockIdx.x, h = b >> 3, s = (b + h) & 7;
  const int tid = threadIdx.x, lane = tid & 63, w = __builtin_amdgcn_readfirstlane(tid >> 6);
  const int r = lane >> 4, c = lane & 15;
  STAMP(0);
  if (tid < 4) L.cnt[tid] = 0u;
  const bool qwave = w < 4, gwave = (w >= 4) && (w < 8) && (h < NGRP);
  const bool p1wave = qwave || gwave;
  if (!p1wave) {
    LGKM0();
    __builtin_amdgcn_s_barrier();
  } else {
    __builtin_amdgcn_s_setprio(3);
    const __amdgpu_buffer_rsrc_t rsx = mkrs(x, D * 2);
    unsigned xr[5]; u32x4 wv[5], xa, xb;
    const int row0 = 80 * s + (qwave ? 20 * w + r : 40 * (w & 1) + (lane >> 3));
    const int rstep = qwave ? 4 : 8, rbytes = qwave ? HQ * DH * 2 : F * 2;
    const int coff = qwave ? h * 256 + c * 16 : h * 128 + (lane & 7) * 16;
    const __amdgpu_buffer_rsrc_t rsw = mkrs(qwave ? wq : (w < 6 ? wg : wu), qwave ? D * HQ * DH * 2 : D * F * 2);
    xa = __builtin_amdgcn_raw_buffer_load_b128(rsx, lane * 16, 0, 0);
    xb = __builtin_amdgcn_raw_buffer_load_b128(rsx, 1024 + lane * 16, 0, 0);
#pragma unroll
    for (int i = 0; i < 5; ++i) wv[i] = ldb(rsw, row0 * rbytes + coff, i * rstep * rbytes);
#pragma unroll
    for (int i = 0; i < 5; ++i) xr[i] = __builtin_amdgcn_raw_buffer_load_b16(rsx, row0 * 2, i * rstep * 2, 0);
    const unsigned ep = __builtin_amdgcn_raw_buffer_load_b32(mkrs(wsp + G_EPOCH, (w & 3) == 0 ? 4 : 0), 0, 0, 16);
    SB();
    LGKM0();
    __builtin_amdgcn_s_barrier();
    SB();
    float sm = 0.f;
#pragma unroll
    for (int k = 0; k < 4; ++k) sm += (lo_f(xa[k]) + hi_f(xa[k])) + (lo_f(xb[k]) + hi_f(xb[k]));
    const float mean = sum64(sm) * (1.0f / D);
    float sq = 0.f, sq2 = 0.f;
#pragma unroll
    for (int k = 0; k < 4; ++k) {
      float d0 = lo_f(xa[k]) - mean, d1 = hi_f(xa[k]) - mean, d2 = lo_f(xb[k]) - mean, d3 = hi_f(xb[k]) - mean;
      sq += d0 * d0 + d1 * d1; sq2 += d2 * d2 + d3 * d3;
    }
    sq += lane < 16 ? sq2 : 0.f;
    const float var = sum64(sq) * (1.0f / D);
    const float mb = rbf(mean), den = rbf(sqrtf(rbf(rbf(var) + 1e-5f)));
    float acc[8];
#pragma unroll
    for (int k = 0; k < 8; ++k) acc[k] = 0.f;
#pragma unroll
    for (int i = 0; i < 5; ++i) { const float xn = rbf(rbf(lo_f(xr[i]) - mb) / den); fma8(acc, wv[i], xn); }
    if (qwave) {
#pragma unroll
      for (int k = 0; k < 8; ++k) acc[k] = x32_sum(x16_sum(acc[k]));
      if (lane < 16) { *(f32x4*)&L.p1[w][8 * c] = f32x4{acc[0], acc[1], acc[2], acc[3]}; *(f32x4*)&L.p1[w][8 * c + 4] = f32x4{acc[4], acc[5], acc[6], acc[7]}; }
    } else {
#pragma unroll
      for (int k = 0; k < 8; ++k) { float t = acc[k]; t += dpp<ROR8>(t); acc[k] = x32_sum(x16_sum(t)); }
      if (lane < 8) { *(f32x4*)&L.p1[w][8 * lane] = f32x4{acc[0], acc[1], acc[2], acc[3]}; *(f32x4*)&L.p1[w][8 * lane + 4] = f32x4{acc[4], acc[5], acc[6], acc[7]}; }
    }
    if ((w & 3) == 0 && lane == 0) L.tagw[w >> 2] = ep * 0x9E3779B1u + (unsigned)dispatch_id_intrin() * 2u + 1u;
    LGKM0();
    STAMP(1);
    unsigned old = 0;
    if (lane == 0) old = __hip_atomic_fetch_add(&L.cnt[qwave ? 0 : 1], 1u, RLX_WG);
    old = __builtin_amdgcn_readfirstlane(old);
    asm volatile("" ::: "memory");
    if (old == 3) {
      const unsigned tag = L.tagw[w >> 2];
      if (qwave) {
        const float v0 = (L.p1[0][lane] + L.p1[1][lane]) + (L.p1[2][lane] + L.p1[3][lane]);
        const float v1 = (L.p1[0][lane + 64] + L.p1[1][lane + 64]) + (L.p1[2][lane + 64] + L.p1[3][lane + 64]);
        gu64* g = ws + G_QP + (h * NS + s) * 128;
        put(g + lane, tag, v0); put(g + 64 + lane, tag, v1);
        STAMP(2);
      } else {
        const float v0 = L.p1[4][lane] + L.p1[5][lane], v1 = L.p1[6][lane] + L.p1[7][lane];
        gu64* g = ws + G_GP + (h * NS + s) * 128;
        put(g + lane, tag, v0); put(g + 64 + lane, tag, v1);
      }
    }
    if (!qwave) __builtin_amdgcn_s_setprio(0);
  }

  const size_t kvoff = ((size_t)h * SK + (SK - WIN) + s * KPS + w * 32) * DH;
  const __amdgpu_buffer_rsrc_t rsk = mkrs(kc + kvoff, 32 * DH * 2), rsv = mkrs(vc + kvoff, 32 * DH * 2);
  u32x4 kr[8], vr[8], w3[2];
#pragma unroll
  for (int i = 0; i < 8; ++i) kr[i] = ldb(rsk, lane * 16, i * 1024);
  SB();
#pragma unroll
  for (int i = 0; i < 8; ++i) vr[i] = ldb(rsv, lane * 16, i * 1024);
  SB();
  const int fj = (HQ - 1 - h) * 3 + (s - 5);
  const bool typeA = s < 5, typeB = !typeA && fj < NFI * 5;
  const int fgi = fj / 5, slice = typeA ? s : fj % 5;
  {
    const u16* wb = typeA ? wo + (size_t)h * DH * D : wd + (size_t)(typeB ? fgi : 0) * 128 * D;
    const unsigned nb = typeA ? DH * D * 2 : (typeB ? (fgi == NFI - 1 ? 64 : 128) * D * 2 : 0);
    const __amdgpu_buffer_rsrc_t rs3 = mkrs(wb, nb);
#pragma unroll
    for (int i = 0; i < 2; ++i) w3[i] = ldb(rs3, (8 * w + 4 * i + r) * (D * 2) + slice * 256 + c * 16, 0);
  }
  SB();
  STAMP(4);
  if (qwave) {
    for (unsigned spin = 0; spin < SPIN_L; ++spin) { if (__hip_atomic_load(&L.cnt[0], RLX_WG) >= 4u) break; __builtin_amdgcn_s_sleep(1); }
    asm volatile("" ::: "memory");
    const unsigned tag = L.tagw[0];
    gu64* g = ws + G_QP + (h * NS + (lane >> 5)) * 128 + 32 * w + (lane & 31);
    unsigned long long t[4];
#pragma unroll
    for (int i = 0; i < 4; ++i) t[i] = get(g + i * 256);
    for (unsigned spin = 0; spin < SPIN_G; ++spin) {
      bool ok = true;
#pragma unroll
      for (int i = 0; i < 4; ++i) ok &= (unsigned)(t[i] >> 32) == tag;
      if (__all(ok)) break;
      __builtin_amdgcn_s_sleep(8);
#pragma unroll
      for (int i = 0; i < 4; ++i) if ((unsigned)(t[i] >> 32) != tag) t[i] = get(g + i * 256);
    }
    const float qv = x32_sum((__uint_as_float((unsigned)t[0]) + __uint_as_float((unsigned)t[1])) + (__uint_as_float((unsigned)t[2]) + __uint_as_float((unsigned)t[3])));
    if (lane < 32) ((u16*)L.qs)[32 * w + lane] = (u16)bf_bits(qv);
    LGKM0();
    if (lane == 0) __hip_atomic_fetch_add(&L.cnt[2], 1u, RLX_WG);
    __builtin_amdgcn_s_setprio(0);
    STAMP(3);
  }
  for (unsigned spin = 0; spin < SPIN_L; ++spin) {
    if (__hip_atomic_load(&L.cnt[2], RLX_WG) >= 4u) break;
    __builtin_amdgcn_s_sleep(1);
  }
  asm volatile("" ::: "memory");
  const u32x4 qp = *(const u32x4*)&L.qs[4 * c];
  const unsigned tag = __builtin_amdgcn_readfirstlane(L.tagw[0]);
  STAMP(5);
  float sc[8];
#pragma unroll
  for (int i = 0; i < 8; ++i) {
    float d = dot2(kr[i][0], qp[0], 0.f);
#pragma unroll
    for (int k = 1; k < 4; ++k) d = dot2(kr[i][k], qp[k], d);
    d = sum16(d);
    sc[i] = rbf(rbf(d) * 0.08838834764831845f);
  }
  float mx = sc[0];
#pragma unroll
  for (int i = 1; i < 8; ++i) mx = fmaxf(mx, sc[i]);
  mx = x32_max(x16_max(mx));
  constexpr float LOG2E = 1.4426950408889634f;
  float ls = 0.f;
#pragma unroll
  for (int i = 0; i < 8; ++i) { sc[i] = __builtin_amdgcn_exp2f((sc[i] - mx) * LOG2E); ls += sc[i]; }
  ls = x32_sum(x16_sum(ls));
  STAMP(6);
  float acc[8];
#pragma unroll
  for (int k = 0; k < 8; ++k) acc[k] = 0.f;
#pragma unroll
  for (int i = 0; i < 8; ++i) fma8(acc, vr[i], sc[i]);
#pragma unroll
  for (int k = 0; k < 8; ++k) acc[k] = x32_sum(x16_sum(acc[k]));
  if (lane < 16) {
    *(f32x4*)&L.red[w][8 * c] = f32x4{acc[0], acc[1], acc[2], acc[3]}; *(f32x4*)&L.red[w][8 * c + 4] = f32x4{acc[4], acc[5], acc[6], acc[7]};
    if (lane == 0) { L.m[w] = mx; L.l[w] = ls; }
  }
  STAMP(7);
  LGKM0();
  __builtin_amdgcn_s_barrier();
  asm volatile("" ::: "memory");
  if (w < 2) {
    float mw[NW], lw[NW];
#pragma unroll
    for (int i = 0; i < NW; i += 4) { const f32x4 a = *(const f32x4*)&L.m[i], bb = *(const f32x4*)&L.l[i];
#pragma unroll
      for (int k = 0; k < 4; ++k) { mw[i + k] = a[k]; lw[i + k] = bb[k]; } }
    float M = mw[0];
#pragma unroll
    for (int i = 1; i < NW; ++i) M = fmaxf(M, mw[i]);
    float a = 0.f, Ls = 0.f;
#pragma unroll
    for (int i = 0; i < NW; ++i) { const float e = __builtin_amdgcn_exp2f((mw[i] - M) * LOG2E); a = fmaf(e, L.red[i][tid], a); Ls = fmaf(e, lw[i], Ls); }
    put(ws + G_AP + (h * NS + s) * 128 + tid, tag, a);
    if (tid < 2) put(ws + G_ML + (h * NS + s) * 2 + tid, tag, tid == 0 ? M : Ls);
  }
  STAMP(8);
  if (typeA || typeB) {
    const int p = lane & 7, j = 4 * ((lane >> 3) & 1) + r;
    float own;
    if (typeA) {
      gu64* ga = ws + G_AP + (h * NS + p) * 128 + 8 * w + j;
      const __amdgpu_buffer_rsrc_t rsm = mkrs(wsp + G_ML + h * NS * 2, NS * 16);
      unsigned long long ta = get(ga);
      u32x4 ml = __builtin_amdgcn_raw_buffer_load_b128(rsm, p * 16, 0, 16);
      for (unsigned spin = 0; spin < SPIN_G; ++spin) {
        const bool ok = (unsigned)(ta >> 32) == tag && ml[1] == tag && ml[3] == tag;
        if (__all(ok)) break;
        __builtin_amdgcn_s_sleep(POLL1);
        asm volatile("" ::: "memory");
        if (!ok) { ta = get(ga); ml = __builtin_amdgcn_raw_buffer_load_b128(rsm, p * 16, 0, 16); }
      }
      const unsigned tm = ml[0], tl = ml[2];
      const float mp = __uint_as_float((unsigned)tm), M = max8(mp);
      const float e = __builtin_amdgcn_exp2f((mp - M) * LOG2E);
      const float Ls = sum8(e * __uint_as_float((unsigned)tl)), a = sum8(e * __uint_as_float((unsigned)ta));
      own = rbf(a / Ls);
    } else {
      const int rr = 8 * w + j, grp = min(2 * fgi + (rr >> 6), NGRP - 1);
      gu64* gg = ws + G_GP + (grp * NS + p) * 128 + (rr & 63);
      unsigned long long tg = get(gg), tu = get(gg + 64);
      for (unsigned spin = 0; spin < SPIN_G; ++spin) {
        const bool ok = (unsigned)(tg >> 32) == tag && (unsigned)(tu >> 32) == tag;
        if (__all(ok)) break;
        __builtin_amdgcn_s_sleep(8);
        if (!ok) { tg = get(gg); tu = get(gg + 64); }
      }
      const float g = rbf(sum8(__uint_as_float((unsigned)tg))), u = rbf(sum8(__uint_as_float((unsigned)tu)));
      const float sg = rbf(1.0f / (1.0f + __expf(-g)));
      own = rbf(sg * u);
    }
    STAMP(9);
    const float oth = dpp<ROR8>(own);
    const bool hi = (lane >> 3) & 1;
    const float o0 = hi ? oth : own, o1 = hi ? own : oth;
    float a3[8];
#pragma unroll
    for (int k = 0; k < 8; ++k) a3[k] = 0.f;
    fma8(a3, w3[0], o0); fma8(a3, w3[1], o1);
#pragma unroll
    for (int k = 0; k < 8; ++k) a3[k] = x32_sum(x16_sum(a3[k]));
    if (lane < 16) { *(f32x4*)&L.red3[w][8 * c] = f32x4{a3[0], a3[1], a3[2], a3[3]}; *(f32x4*)&L.red3[w][8 * c + 4] = f32x4{a3[4], a3[5], a3[6], a3[7]}; }
    LGKM0();
    __builtin_amdgcn_s_barrier();
    asm volatile("" ::: "memory");
    if (w < 2) {
      float a = 0.f;
#pragma unroll
      for (int i = 0; i < NW; ++i) a += L.red3[i][tid];
      put(ws + G_FP + (slice * NSRC + (typeA ? h : HQ + fgi)) * 128 + tid, tag, a);
    }
    STAMP(10);
  }
  const int rj = h * 3 + (s - 5);
  if (!typeA && rj < 80 && w == 2) {
    const int p = lane >> 3, cc = lane & 7, col = 8 * rj + cc;
    gu64* g = ws + G_FP + ((rj >> 4) * NSRC + p) * 128 + 8 * (rj & 15) + cc;
    const float xv = lo_f(x[col]);
    unsigned long long t[6];
#pragma unroll
    for (int i = 0; i < 6; ++i) t[i] = (8 * i + p < NSRC) ? get(g + i * 8 * 128) : ((unsigned long long)tag << 32);
    for (unsigned spin = 0; spin < SPIN_G; ++spin) {
      bool ok = true;
#pragma unroll
      for (int i = 0; i < 6; ++i) ok &= (unsigned)(t[i] >> 32) == tag;
      if (__all(ok)) break;
      __builtin_amdgcn_s_sleep(POLL2);
#pragma unroll
      for (int i = 0; i < 6; ++i) if ((unsigned)(t[i] >> 32) != tag) t[i] = get(g + i * 8 * 128);
    }
    float v[6];
#pragma unroll
    for (int i = 0; i < 6; ++i) v[i] = __uint_as_float((unsigned)t[i]);
    float a = (v[0] + v[1]) + (v[2] + v[3]), bb = v[4] + v[5];
    a += dpp<ROR8>(a); a = x32_sum(x16_sum(a));
    bb += dpp<ROR8>(bb); bb = x32_sum(x16_sum(bb));
    const float res = rbf(xv + rbf(a)) + rbf(bb);
    if (lane < 8) out[col] = (u16)bf_bits(res);
    if (rj == 0 && lane == 0) __hip_atomic_store((gu32*)(ws + G_EPOCH), tag, RLX_AGENT);
    STAMP(11);
  }
  while (__builtin_amdgcn_s_memrealtime() - tstart < 6000ull) __builtin_amdgcn_s_sleep(2);
}

extern "C" void kernel_launch(void* const* d_in, const int* in_sizes, int n_in, void* d_out, int out_size, void* d_ws, size_t ws_size, hipStream_t stream) {
  fused_kernel<<<256, 1024, 0, stream>>>((const u16*)d_in[0], (const u16*)d_in[1], (const u16*)d_in[2], (const u16*)d_in[3], (const u16*)d_in[4],
                                         (const u16*)d_in[6], (const u16*)d_in[7], (const u16*)d_in[8], (u16*)d_out, (unsigned long long*)d_ws);
}
```

```cpp
#include <hip/hip_runtime.h>
#include <hip/hip_bf16.h>
#include <cstdint>

extern "C" __device__ unsigned long long dispatch_id_intrin() __asm("llvm.amdgcn.dispatch.id");
typedef unsigned short u16;
typedef unsigned u32x4 __attribute__((ext_vector_type(4)));
typedef unsigned u32x8 __attribute__((ext_vector_type(8)));
typedef float f32x4 __attribute__((ext_vector_type(4)));
typedef __bf16 bf16x2 __attribute__((ext_vector_type(2)));
typedef __bf16 bf16x8 __attribute__((ext_vector_type(8)));
typedef __attribute__((address_space(1))) unsigned long long gu64;
typedef __attribute__((address_space(1))) unsigned gu32;
#define RLX_AGENT __ATOMIC_RELAXED, __HIP_MEMORY_SCOPE_AGENT
#define RLX_WG __ATOMIC_RELAXED, __HIP_MEMORY_SCOPE_WORKGROUP

constexpr int D = 640, HQ = 32, DH = 128, SK = 32768, WIN = 4096, F = 1728;
constexpr int NS = 8, KPS = WIN / NS;
constexpr int NW = 12, NH = 4;
constexpr int NGRP = F / 64;
constexpr int NFI = 14;
constexpr int NSRC = HQ + NFI;
constexpr int AUX_STREAM = 19;
constexpr int OOB = 0x40000000;
constexpr int G_EPOCH = 0;
constexpr int G_QP = 64;
constexpr int G_GP = G_QP + HQ * NS * 128;
constexpr int AP_H = NS * 128 + 16;
constexpr int G_AP = G_GP + NGRP * NS * 128;
constexpr int G_FP = G_AP + HQ * AP_H;
constexpr int G_END = G_FP + 80 * 48 * 8;
#define QDELAY 16
#define PGAP 4
constexpr unsigned SPIN_L = 1u << 22;

#define STAMP(i) do {} while (0)
#define STAMP_OUT() do {} while (0)

__device__ __forceinline__ float lo_f(unsigned v) { return __uint_as_float(v << 16); }
__device__ __forceinline__ float hi_f(unsigned v) { return __uint_as_float(v & 0xffff0000u); }
__device__ __forceinline__ float rbf(float f) { return (float)(__bf16)f; }
__device__ __forceinline__ unsigned pk2(float a, float b) { bf16x2 v = {(__bf16)a, (__bf16)b}; return __builtin_bit_cast(unsigned, v); }
__device__ __forceinline__ float dot2(unsigned a, unsigned b, float acc) {
  return __builtin_amdgcn_fdot2_f32_bf16(__builtin_bit_cast(bf16x2, a), __builtin_bit_cast(bf16x2, b), acc, false);
}
template <int CTRL> __device__ __forceinline__ unsigned dppu(unsigned x) { return (unsigned)__builtin_amdgcn_mov_dpp((int)x, CTRL, 0xf, 0xf, true); }
template <int CTRL> __device__ __forceinline__ float dpp(float x) { return __uint_as_float(dppu<CTRL>(__float_as_uint(x))); }
constexpr int XOR1 = 0xB1, XOR2 = 0x4E, HMIR = 0x141, ROR4 = 0x124, ROR8 = 0x128, ROR12 = 0x12C, BCAST = 0x150;
__device__ __forceinline__ float x16_sum(float x) { auto s = __builtin_amdgcn_permlane16_swap(__float_as_uint(x), __float_as_uint(x), false, false); return __uint_as_float(s[0]) + __uint_as_float(s[1]); }
__device__ __forceinline__ float x32_sum(float x) { auto s = __builtin_amdgcn_permlane32_swap(__float_as_uint(x), __float_as_uint(x), false, false); return __uint_as_float(s[0]) + __uint_as_float(s[1]); }
__device__ __forceinline__ float x16_max(float x) { auto s = __builtin_amdgcn_permlane16_swap(__float_as_uint(x), __float_as_uint(x), false, false); return fmaxf(__uint_as_float(s[0]), __uint_as_float(s[1])); }
__device__ __forceinline__ float x32_max(float x) { auto s = __builtin_amdgcn_permlane32_swap(__float_as_uint(x), __float_as_uint(x), false, false); return fmaxf(__uint_as_float(s[0]), __uint_as_float(s[1])); }
__device__ __forceinline__ float rows_sum(float x) { return x32_sum(x16_sum(x)); }
__device__ __forceinline__ float rows_max(float x) { return x32_max(x16_max(x)); }
__device__ __forceinline__ float sum16(float x) { x += dpp<XOR1>(x); x += dpp<XOR2>(x); x += dpp<HMIR>(x); x += dpp<ROR8>(x); return x; }
__device__ __forceinline__ float max16(float x) { x = fmaxf(x, dpp<XOR1>(x)); x = fmaxf(x, dpp<XOR2>(x)); x = fmaxf(x, dpp<HMIR>(x)); x = fmaxf(x, dpp<ROR8>(x)); return x; }
__device__ __forceinline__ float sum64(float x) { return rows_sum(sum16(x)); }
__device__ __forceinline__ f32x4 mfma_rows(f32x4 acc, u32x4 w, unsigned pp, u32x4 msk) {
  const u32x4 a = {pp & msk[0], pp & msk[1], pp & msk[2], pp & msk[3]};
  return __builtin_amdgcn_mfma_f32_16x16x32_bf16(__builtin_bit_cast(bf16x8, a), __builtin_bit_cast(bf16x8, w), acc, 0, 0, 0);
}
__device__ __forceinline__ __amdgpu_buffer_rsrc_t mkrs(const void* p, unsigned bytes) { return __builtin_amdgcn_make_buffer_rsrc((void*)p, 0, bytes, 0x00020000); }
__device__ __forceinline__ u32x4 ldb(__amdgpu_buffer_rsrc_t rs, int voff, int soff) { return __builtin_amdgcn_raw_buffer_load_b128(rs, voff, soff, AUX_STREAM); }
__device__ __forceinline__ void put(gu64* g, unsigned tag, float v) { __hip_atomic_store(g, ((unsigned long long)tag << 32) | __float_as_uint(v), RLX_AGENT); }
#define LGKM0() asm volatile("s_waitcnt lgkmcnt(0)" ::: "memory")
#define SB() __builtin_amdgcn_sched_barrier(0)
#define CBAR() asm volatile("" ::: "memory")

#define STR_(x) #x
#define STR(x) STR_(x)
#define POLL_CHECK(N, B0, B1, B2, B3, T0, T1, OFF, HAVE) \
  "s_waitcnt vmcnt(" #N ")\n\t" \
  "v_cmp_eq_u32 vcc, %[tag], v" #B1 "\n\t" \
  "v_cmp_eq_u32 %[tmp], %[tag], v" #B3 "\n\t" \
  "s_and_b64 vcc, vcc, %[tmp]\n\t" \
  "s_andn2_b64 vcc, vcc, %[" #HAVE "]\n\t" \
  "v_cndmask_b32 %[" #T0 "], %[" #T0 "], v" #B0 ", vcc\n\t" \
  "v_cndmask_b32 %[" #T1 "], %[" #T1 "], v" #B2 ", vcc\n\t" \
  "v_cndmask_b32 %[" #OFF "], %[" #OFF "], %[oob], vcc\n\t" \
  "s_or_b64 %[" #HAVE "], %[" #HAVE "], vcc\n\t"
#define POLL_LOAD(B0, B3, OFF) "buffer_load_dwordx4 v[" #B0 ":" #B3 "], %[" #OFF "], %[rs], 0 offen sc1\n\t"
#define POLL_TAIL "s_sub_u32 %[cnt], %[cnt], 1\n\ts_cmp_lg_u32 %[cnt], 0\n\ts_cbranch_scc1 1b\n2:\n\t"
struct Scratch { u32x8 a, b, c; };
#define POLL_DRAIN(S) asm volatile("s_waitcnt vmcnt(0)" : "+{v[136:143]}"(S.a), "+{v[144:151]}"(S.b), "+{v[152:159]}"(S.c) :: "memory")
__device__ __forceinline__ void poll2(Scratch& S, __amdgpu_buffer_rsrc_t rs, unsigned tag, int of0, int of1, float (&v)[4]) {
  unsigned a0 = 0u, a1 = 0u, b0 = 0u, b1 = 0u, cnt; unsigned long long hv0, hv1, tmp;
#define DONE2 "s_and_b64 %[tmp], %[hv0], %[hv1]\n\ts_cmp_eq_u64 %[tmp], -1\n\ts_cbranch_scc1 2f\n\t"
  asm volatile(
    "s_nop 4\n\t"
    POLL_LOAD(136, 139, of0) POLL_LOAD(140, 143, of1) "s_sleep " STR(PGAP) "\n\t"
    POLL_LOAD(144, 147, of0) POLL_LOAD(148, 151, of1) "s_sleep " STR(PGAP) "\n\t"
    POLL_LOAD(152, 155, of0) POLL_LOAD(156, 159, of1)
    "s_mov_b64 %[hv0], 0\n\ts_mov_b64 %[hv1], 0\n\ts_mov_b32 %[cnt], 0x20000\n"
    "1:\n\t"
    POLL_CHECK(5, 136, 137, 138, 139, a0, a1, of0, hv0) DONE2 POLL_LOAD(136, 139, of0)
    POLL_CHECK(5, 140, 141, 142, 143, b0, b1, of1, hv1) DONE2 POLL_LOAD(140, 143, of1)
    POLL_CHECK(5, 144, 145, 146, 147, a0, a1, of0, hv0) DONE2 POLL_LOAD(144, 147, of0)
    POLL_CHECK(5, 148, 149, 150, 151, b0, b1, of1, hv1) DONE2 POLL_LOAD(148, 151, of1)
    POLL_CHECK(5, 152, 153, 154, 155, a0, a1, of0, hv0) DONE2 POLL_LOAD(152, 155, of0)
    POLL_CHECK(5, 156, 157, 158, 159, b0, b1, of1, hv1) DONE2 POLL_LOAD(156, 159, of1)
    POLL_TAIL
    : [a0] "+v"(a0), [a1] "+v"(a1), [b0] "+v"(b0), [b1] "+v"(b1), [of0] "+v"(of0), [of1] "+v"(of1),
      [hv0] "=&s"(hv0), [hv1] "=&s"(hv1), [tmp] "=&s"(tmp), [cnt] "=&s"(cnt),
      "=&{v[136:143]}"(S.a), "=&{v[144:151]}"(S.b), "=&{v[152:159]}"(S.c)
    : [rs] "s"(rs), [tag] "s"(tag), [oob] "v"(OOB)
    : "vcc", "scc", "memory");
#undef DONE2
  v[0] = __uint_as_float(a0); v[1] = __uint_as_float(a1); v[2] = __uint_as_float(b0); v[3] = __uint_as_float(b1);
}
__device__ __forceinline__ void poll2s(__amdgpu_buffer_rsrc_t rs, unsigned tag, int of0, int of1, float (&v)[4]) {
  unsigned a0 = 0u, a1 = 0u, b0 = 0u, b1 = 0u, cnt; unsigned long long hv0, hv1, tmp;
  asm volatile(
    "s_nop 4\n\t"
    "s_mov_b64 %[hv0], 0\n\ts_mov_b64 %[hv1], 0\n\ts_mov_b32 %[cnt], 0x40000\n"
    "1:\n\t"
    POLL_LOAD(136, 139, of0) POLL_LOAD(140, 143, of1)
    POLL_CHECK(1, 136, 137, 138, 139, a0, a1, of0, hv0)
    POLL_CHECK(0, 140, 141, 142, 143, b0, b1, of1, hv1)
    "s_and_b64 %[tmp], %[hv0], %[hv1]\n\ts_cmp_eq_u64 %[tmp], -1\n\ts_cbranch_scc1 2f\n\t"
    POLL_TAIL
    : [a0] "+v"(a0), [a1] "+v"(a1), [b0] "+v"(b0), [b1] "+v"(b1), [of0] "+v"(of0), [of1] "+v"(of1),
      [hv0] "=&s"(hv0), [hv1] "=&s"(hv1), [tmp] "=&s"(tmp), [cnt] "=&s"(cnt)
    : [rs] "s"(rs), [tag] "s"(tag), [oob] "v"(OOB)
    : "vcc", "scc", "memory", "v136", "v137", "v138", "v139", "v140", "v141", "v142", "v143");
  v[0] = __uint_as_float(a0); v[1] = __uint_as_float(a1); v[2] = __uint_as_float(b0); v[3] = __uint_as_float(b1);
}
__device__ __forceinline__ void poll3(Scratch& S, __amdgpu_buffer_rsrc_t rs, unsigned tag, int of0, int of1, int of2, unsigned long long hv2, float (&v)[6]) {
  unsigned a0 = 0u, a1 = 0u, b0 = 0u, b1 = 0u, c0 = 0u, c1 = 0u, cnt; unsigned long long hv0, hv1, tmp;
#define DONE3 "s_and_b64 %[tmp], %[hv0], %[hv1]\n\ts_and_b64 %[tmp], %[tmp], %[hv2]\n\ts_cmp_eq_u64 %[tmp], -1\n\ts_cbranch_scc1 2f\n\t"
  asm volatile(
    "s_nop 4\n\t"
    POLL_LOAD(136, 139, of0) POLL_LOAD(140, 143, of1) POLL_LOAD(144, 147, of2) "s_sleep " STR(PGAP) "\n\t"
    POLL_LOAD(148, 151, of0) POLL_LOAD(152, 155, of1) POLL_LOAD(156, 159, of2)
    "s_mov_b64 %[hv0], 0\n\ts_mov_b64 %[hv1], 0\n\ts_mov_b32 %[cnt], 0x20000\n"
    "1:\n\t"
    POLL_CHECK(5, 136, 137, 138, 139, a0, a1, of0, hv0) DONE3 POLL_LOAD(136, 139, of0)
    POLL_CHECK(5, 140, 141, 142, 143, b0, b1, of1, hv1) DONE3 POLL_LOAD(140, 143, of1)
    POLL_CHECK(5, 144, 145, 146, 147, c0, c1, of2, hv2) DONE3 POLL_LOAD(144, 147, of2)
    POLL_CHECK(5, 148, 149, 150, 151, a0, a1, of0, hv0) DONE3 POLL_LOAD(148, 151, of0)
    POLL_CHECK(5, 152, 153, 154, 155, b0, b1, of1, hv1) DONE3 POLL_LOAD(152, 155, of1)
    POLL_CHECK(5, 156, 157, 158, 159, c0, c1, of2, hv2) DONE3 POLL_LOAD(156, 159, of2)
    POLL_TAIL
    : [a0] "+v"(a0), [a1] "+v"(a1), [b0] "+v"(b0), [b1] "+v"(b1), [c0] "+v"(c0), [c1] "+v"(c1), [of0] "+v"(of0), [of1] "+v"(of1), [of2] "+v"(of2),
      [hv0] "=&s"(hv0), [hv1] "=&s"(hv1), [hv2] "+s"(hv2), [tmp] "=&s"(tmp), [cnt] "=&s"(cnt),
      "=&{v[136:143]}"(S.a), "=&{v[144:151]}"(S.b), "=&{v[152:159]}"(S.c)
    : [rs] "s"(rs), [tag] "s"(tag), [oob] "v"(OOB)
    : "vcc", "scc", "memory");
#undef DONE3
  v[0] = __uint_as_float(a0); v[1] = __uint_as_float(a1); v[2] = __uint_as_float(b0); v[3] = __uint_as_float(b1); v[4] = __uint_as_float(c0); v[5] = __uint_as_float(c1);
}

struct Lds {
  float red[16][128];
  float p1q[NH][128], p1g[NH][128], red3[NH][128];
  float m[16], l[16];
  unsigned qs[64];
  unsigned cnt[8];
};

__device__ __forceinline__ void lds_wait(unsigned* p, unsigned want) {
  for (unsigned spin = 0; spin < SPIN_L; ++spin) {
    if (__hip_atomic_load(p, RLX_WG) >= want) break;
    __builtin_amdgcn_s_sleep(1);
  }
  CBAR();
}
constexpr float LOG2E = 1.4426950408889634f;

__device__ __forceinline__ void attend32(const u32x4 (&kr)[8], const u32x4 (&vr)[8], u32x4 qp, u32x4 msk, int lane, float* red, float* mo, float* lo) {
  const int r = lane >> 4, c = lane & 15;
  float d[8];
#pragma unroll
  for (int i = 0; i < 8; ++i) {
    d[i] = dot2(kr[i][0], qp[0], 0.f);
#pragma unroll
    for (int k = 1; k < 4; ++k) d[i] = dot2(kr[i][k], qp[k], d[i]);
  }
  const bool c3 = c & 8, c2 = c & 4;
#pragma unroll
  for (int i = 0; i < 4; ++i) { const float keep = c3 ? d[i + 4] : d[i], send = c3 ? d[i] : d[i + 4]; d[i] = keep + dpp<ROR8>(send); }
#pragma unroll
  for (int i = 0; i < 2; ++i) { const float keep = c2 ? d[i + 2] : d[i], send = c2 ? d[i] : d[i + 2]; d[i] = keep + dpp<HMIR>(send); }
#pragma unroll
  for (int i = 0; i < 2; ++i) { d[i] += dpp<XOR2>(d[i]); d[i] += dpp<XOR1>(d[i]); }
  const float s0 = rbf(rbf(d[0]) * 0.08838834764831845f), s1 = rbf(rbf(d[1]) * 0.08838834764831845f);
  float mx = fmaxf(s0, s1);
  mx = fmaxf(mx, dpp<HMIR>(mx)); mx = fmaxf(mx, dpp<ROR8>(mx));
  mx = rows_max(mx);
  const float e0 = __builtin_amdgcn_exp2f((s0 - mx) * LOG2E), e1 = __builtin_amdgcn_exp2f((s1 - mx) * LOG2E);
  const unsigned pp0 = pk2(e0, e0), pp1 = pk2(e1, e1);
  float ls = e0 + e1;
  ls += dpp<HMIR>(ls); ls += dpp<ROR8>(ls);
  ls = rows_sum(ls);
  if (lane == 0) { *mo = mx; *lo = ls; }
  f32x4 acc = {0.f, 0.f, 0.f, 0.f};
  acc = mfma_rows(acc, vr[0], dppu<BCAST + 0>(pp0), msk);
  acc = mfma_rows(acc, vr[1], dppu<BCAST + 0>(pp1), msk);
  acc = mfma_rows(acc, vr[2], dppu<BCAST + 4>(pp0), msk);
  acc = mfma_rows(acc, vr[3], dppu<BCAST + 4>(pp1), msk);
  acc = mfma_rows(acc, vr[4], dppu<BCAST + 8>(pp0), msk);
  acc = mfma_rows(acc, vr[5], dppu<BCAST + 8>(pp1), msk);
  acc = mfma_rows(acc, vr[6], dppu<BCAST + 12>(pp0), msk);
  acc = mfma_rows(acc, vr[7], dppu<BCAST + 12>(pp1), msk);
  if (lane < 32) *(f32x4*)&red[8 * c + 4 * r] = acc;
}

__global__ void __launch_bounds__(NW * 64) fused_kernel(const u16* __restrict__ x, const u16* __restrict__ kc, const u16* __restrict__ vc,
                                                        const u16* __restrict__ wq, const u16* __restrict__ wo, const u16* __restrict__ wg,
                                                        const u16* __restrict__ wu, const u16* __restrict__ wd, u16* __restrict__ out,
                                                        unsigned long long* __restrict__ wsp) {
  __shared__ __attribute__((aligned(16))) Lds L;
  gu64* ws = (gu64*)wsp;
  const int b = blockIdx.x, h = b >> 3, s = (b + h) & 7;
  const int tid = threadIdx.x, lane = tid & 63, w = __builtin_amdgcn_readfirstlane(tid >> 6);
  const int r = lane >> 4, c = lane & 15;
  STAMP(0);
  if (tid < 8) L.cnt[tid] = 0u;
  asm volatile("" :: "s"(x), "s"(kc), "s"(vc), "s"(wq), "s"(wo), "s"(wg), "s"(wu), "s"(wd), "s"(out), "s"(wsp));
  LGKM0();
  u32x4 msk;
#pragma unroll
  for (int t = 0; t < 4; ++t) msk[t] = (c >> 1) == t ? ((c & 1) ? 0xffff0000u : 0x0000ffffu) : 0u;

  unsigned ep;
  asm volatile("s_load_dword %0, %1, 0x0" : "=s"(ep) : "s"(wsp) : "memory");
#define MAKE_TAG() ({ asm volatile("s_waitcnt lgkmcnt(0)" : "+s"(ep) :: "memory"); ep * 0x9E3779B1u + (unsigned)dispatch_id_intrin() * 0x85EBCA77u + 0x27D4EB2Fu; })
  const int fj = (HQ - 1 - h) * 3 + (s - 5);
  const bool typeA = s < 5, typeB = !typeA && fj < NFI * 5;
  const int fgi = fj / 5, slice = typeA ? s : fj % 5;
  Scratch S;
  if (w >= NH) {
    const int j = w - NH, ca = (j >> 2) * 8 + (j & 3), cb = ca + 4;
    {
      const size_t kvoff = ((size_t)h * SK + (SK - WIN) + s * KPS) * DH;
      const __amdgpu_buffer_rsrc_t rsk = mkrs(kc + kvoff, KPS * DH * 2), rsv = mkrs(vc + kvoff, KPS * DH * 2);
      __builtin_amdgcn_s_barrier();
      u32x4 ka[8], va[8], kb[8], vb[8];
#pragma unroll
      for (int i = 0; i < 8; ++i) ka[i] = ldb(rsk, lane * 16, ca * 8192 + i * 1024);
      SB();
#pragma unroll
      for (int i = 0; i < 8; ++i) va[i] = ldb(rsv, lane * 16, ca * 8192 + i * 1024);
      SB();
#pragma unroll
      for (int i = 0; i < 8; ++i) kb[i] = ldb(rsk, lane * 16, cb * 8192 + i * 1024);
      SB();
#pragma unroll
      for (int i = 0; i < 8; ++i) vb[i] = ldb(rsv, lane * 16, cb * 8192 + i * 1024);
      SB();
      if (lane == 0) __hip_atomic_fetch_add(&L.cnt[3], 1u, RLX_WG);
      STAMP(4);
      lds_wait(&L.cnt[2], NH);
      const u32x4 qp = *(const u32x4*)&L.qs[4 * c];
      STAMP(5);
      attend32(ka, va, qp, msk, lane, L.red[ca], &L.m[ca], &L.l[ca]);
      STAMP(6);
      attend32(kb, vb, qp, msk, lane, L.red[cb], &L.m[cb], &L.l[cb]);
    }
    const unsigned tag = MAKE_TAG();
    if (lane == 0) __hip_atomic_fetch_add(&L.cnt[4], 1u, RLX_WG);
    STAMP(7);
    if (j < 4 && (typeA || typeB)) {
      u32x4 w3[8];
      lds_wait(&L.cnt[3], NW - NH);
      const __amdgpu_buffer_rsrc_t rs3 = mkrs(typeA ? wo + (size_t)h * DH * D : wd + (size_t)fgi * 128 * D, (typeB && fgi == NFI - 1 ? 64 : 128) * D * 2);
#pragma unroll
      for (int i = 0; i < 8; ++i) w3[i] = ldb(rs3, (32 * j + 8 * r) * (D * 2) + slice * 256 + c * 16, i * D * 2);
      SB();
      float o0, o1;
      if (typeB) {
        const int grp = min(2 * fgi + (j >> 1), NGRP - 1);
        const __amdgpu_buffer_rsrc_t rsg = mkrs(wsp + G_GP + grp * NS * 128, NS * 1024);
        const int go = r * 1024 + (j & 1) * 256 + c * 16;
        float g[4], u[4];
        poll2s(rsg, tag, go, go + 4096, g);
        poll2s(rsg, tag, go + 512, go + 4096 + 512, u);
        const float g0 = rbf(rows_sum(g[0] + g[2])), g1 = rbf(rows_sum(g[1] + g[3])), u0 = rbf(rows_sum(u[0] + u[2])), u1 = rbf(rows_sum(u[1] + u[3]));
        o0 = rbf(1.0f / (1.0f + __expf(-g0))) * u0; o1 = rbf(1.0f / (1.0f + __expf(-g1))) * u1;
      } else {
#pragma unroll
        for (int i = 0; i < 8; ++i) asm volatile("" : "+v"(w3[i]));
        lds_wait(&L.cnt[4], NW - NH);
        const int gof = r * 1024 + j * 256 + c * 16;
        float v[6];
        poll3(S, mkrs(wsp + G_AP + h * AP_H, AP_H * 8), tag, gof, gof + 4096, NS * 1024 + (r + 4 * (c >> 3)) * 16, 0ull, v);
        const bool hi = c & 8;
        const float my = dpp<ROR8>(v[4]), ly = dpp<ROR8>(v[5]);
        const float m0 = hi ? my : v[4], l0 = hi ? ly : v[5], m1 = hi ? v[4] : my, l1 = hi ? v[5] : ly;
        const float M = rows_max(fmaxf(m0, m1));
        const float e0 = __builtin_amdgcn_exp2f((m0 - M) * LOG2E), e1 = __builtin_amdgcn_exp2f((m1 - M) * LOG2E);
        const float Ls = rows_sum(fmaf(e0, l0, e1 * l1));
        o0 = rows_sum(fmaf(e0, v[0], e1 * v[2])) / Ls; o1 = rows_sum(fmaf(e0, v[1], e1 * v[3])) / Ls;
      }
      STAMP(9);
      unsigned p0 = pk2(o0, o0), p1 = pk2(o1, o1);
      p0 = __builtin_amdgcn_update_dpp(p0, p0, ROR12, 0x2, 0xf, false); p1 = __builtin_amdgcn_update_dpp(p1, p1, ROR12, 0x2, 0xf, false);
      p0 = __builtin_amdgcn_update_dpp(p0, p0, ROR8, 0x4, 0xf, false);  p1 = __builtin_amdgcn_update_dpp(p1, p1, ROR8, 0x4, 0xf, false);
      p0 = __builtin_amdgcn_update_dpp(p0, p0, ROR4, 0x8, 0xf, false);  p1 = __builtin_amdgcn_update_dpp(p1, p1, ROR4, 0x8, 0xf, false);
      f32x4 a3 = {0.f, 0.f, 0.f, 0.f}, a4 = {0.f, 0.f, 0.f, 0.f};
      a3 = mfma_rows(a3, w3[0], dppu<BCAST + 0>(p0), msk); a4 = mfma_rows(a4, w3[1], dppu<BCAST + 0>(p1), msk);
      a3 = mfma_rows(a3, w3[2], dppu<BCAST + 1>(p0), msk); a4 = mfma_rows(a4, w3[3], dppu<BCAST + 1>(p1), msk);
      a3 = mfma_rows(a3, w3[4], dppu<BCAST + 2>(p0), msk); a4 = mfma_rows(a4, w3[5], dppu<BCAST + 2>(p1), msk);
      a3 = mfma_rows(a3, w3[6], dppu<BCAST + 3>(p0), msk); a4 = mfma_rows(a4, w3[7], dppu<BCAST + 3>(p1), msk);
      if (lane < 32) *(f32x4*)&L.red3[j][8 * c + 4 * r] = a3 + a4;
      LGKM0();
      unsigned old = 0;
      if (lane == 0) old = __hip_atomic_fetch_add(&L.cnt[5], 1u, RLX_WG);
      old = __builtin_amdgcn_readfirstlane(old);
      CBAR();
      if (old == 3) {
        const float v0 = (L.red3[0][lane] + L.red3[1][lane]) + (L.red3[2][lane] + L.red3[3][lane]);
        const float v1 = (L.red3[0][lane + 64] + L.red3[1][lane + 64]) + (L.red3[2][lane + 64] + L.red3[3][lane + 64]);
        gu64* g = ws + G_FP + ((slice * 16 + (lane >> 3)) * 48 + (typeA ? h : HQ + fgi)) * 8 + (lane & 7);
        put(g, tag, v0); put(g + 8 * 48 * 8, tag, v1);
        STAMP(10);
      }
      if (typeA) POLL_DRAIN(S);
    }
    STAMP_OUT();
    return;
  }

  const bool hasg = h < NGRP;
  {
    const __amdgpu_buffer_rsrc_t rsx = mkrs(x, D * 2);
    const int row0 = 80 * s + 20 * w + r;
    const u32x4 xa = __builtin_amdgcn_raw_buffer_load_b128(rsx, lane * 16, 0, 0);
    const u32x4 xb = __builtin_amdgcn_raw_buffer_load_b128(rsx, 1024 + lane * 16, 0, 0);
    unsigned xr[5]; u32x4 wv[5], gv[5];
#pragma unroll
    for (int i = 0; i < 5; ++i) xr[i] = __builtin_amdgcn_raw_buffer_load_b16(rsx, row0 * 2, i * 8, 0);
    const __amdgpu_buffer_rsrc_t rsw = mkrs(wq, D * HQ * DH * 2);
#pragma unroll
    for (int i = 0; i < 5; ++i) wv[i] = ldb(rsw, row0 * (HQ * DH * 2) + h * 256 + c * 16, i * 4 * (HQ * DH * 2));
    const u16* gp = (c < 8 ? wg : wu) + (hasg ? row0 * F + h * 64 + (c & 7) * 8 : 0);
    const int gstep = hasg ? 4 * F : 0;
#pragma unroll
    for (int i = 0; i < 5; ++i) gv[i] = __builtin_nontemporal_load((const u32x4*)(gp + i * gstep));
    SB();
    __builtin_amdgcn_s_barrier();
    SB();
    float sm = 0.f;
#pragma unroll
    for (int k = 0; k < 4; ++k) sm += (lo_f(xa[k]) + hi_f(xa[k])) + (lo_f(xb[k]) + hi_f(xb[k]));
    const float mean = sum64(sm) * (1.0f / D);
    float sq = 0.f, sq2 = 0.f;
#pragma unroll
    for (int k = 0; k < 4; ++k) {
      float d0 = lo_f(xa[k]) - mean, d1 = hi_f(xa[k]) - mean, d2 = lo_f(xb[k]) - mean, d3 = hi_f(xb[k]) - mean;
      sq += d0 * d0 + d1 * d1; sq2 += d2 * d2 + d3 * d3;
    }
    sq += lane < 16 ? sq2 : 0.f;
    const float var = sum64(sq) * (1.0f / D);
    const float mb = rbf(mean), den = rbf(sqrtf(rbf(rbf(var) + 1e-5f)));
    unsigned xp[5];
#pragma unroll
    for (int i = 0; i < 5; ++i) { const float xn = rbf(lo_f(xr[i]) - mb) / den; xp[i] = pk2(xn, xn); }
    f32x4 aq = {0.f, 0.f, 0.f, 0.f};
#pragma unroll
    for (int i = 0; i < 5; ++i) aq = mfma_rows(aq, wv[i], xp[i], msk);
    if (lane < 32) *(f32x4*)&L.p1q[w][8 * c + 4 * r] = aq;
    ep = MAKE_TAG();
    STAMP(1);
    unsigned old = 0;
    if (lane == 0) old = __hip_atomic_fetch_add(&L.cnt[0], 1u, RLX_WG);
    old = __builtin_amdgcn_readfirstlane(old);
    CBAR();
    if (old == NH - 1) {
      const float v0 = (L.p1q[0][lane] + L.p1q[1][lane]) + (L.p1q[2][lane] + L.p1q[3][lane]);
      const float v1 = (L.p1q[0][lane + 64] + L.p1q[1][lane + 64]) + (L.p1q[2][lane + 64] + L.p1q[3][lane + 64]);
      gu64* g = ws + G_QP + (h * NS + s) * 128;
      put(g + lane, ep, v0); put(g + 64 + lane, ep, v1);
      STAMP(2);
    }
    if (hasg) {
      f32x4 ag = {0.f, 0.f, 0.f, 0.f};
#pragma unroll
      for (int i = 0; i < 5; ++i) ag = mfma_rows(ag, gv[i], xp[i], msk);
      if (lane < 32) *(f32x4*)&L.p1g[w][8 * c + 4 * r] = ag;
      LGKM0();
      old = 0;
      if (lane == 0) old = __hip_atomic_fetch_add(&L.cnt[1], 1u, RLX_WG);
      old = __builtin_amdgcn_readfirstlane(old);
      CBAR();
      if (old == NH - 1) {
        const float v0 = (L.p1g[0][lane] + L.p1g[1][lane]) + (L.p1g[2][lane] + L.p1g[3][lane]);
        const float v1 = (L.p1g[0][lane + 64] + L.p1g[1][lane + 64]) + (L.p1g[2][lane + 64] + L.p1g[3][lane + 64]);
        gu64* g = ws + G_GP + (h * NS + s) * 128;
        put(g + lane, ep, v0); put(g + 64 + lane, ep, v1);
      }
    }
  }
  const unsigned tag = ep;
  const int gof = r * 1024 + w * 256 + c * 16;
  {
    __builtin_amdgcn_s_sleep(QDELAY);
    float v[4];
    poll2s(mkrs(wsp + G_QP + h * NS * 128, NS * 1024), tag, gof, gof + 4096, v);
    const float q0 = rows_sum(v[0] + v[2]), q1 = rows_sum(v[1] + v[3]);
    if (lane < 16) L.qs[16 * w + c] = pk2(q0, q1);
    LGKM0();
    if (lane == 0) __hip_atomic_fetch_add(&L.cnt[2], 1u, RLX_WG);
    STAMP(3);
  }
  if (w < 3) {
    lds_wait(&L.cnt[4], NW - NH);
    STAMP(7);
    const float mw = L.m[c], lw = L.l[c];
    float rv[16];
    if (w < 2) {
#pragma unroll
      for (int i = 0; i < 16; ++i) rv[i] = L.red[i][tid];
    }
    const float M = max16(mw);
    const float e = __builtin_amdgcn_exp2f((mw - M) * LOG2E);
    gu64* g = ws + G_AP + h * AP_H;
    if (w < 2) {
      float a0 = 0.f, a1 = 0.f;
#define MRG(i) a0 = fmaf(dpp<BCAST + (i)>(e), rv[i], a0); a1 = fmaf(dpp<BCAST + (i) + 1>(e), rv[(i) + 1], a1);
      MRG(0) MRG(2) MRG(4) MRG(6) MRG(8) MRG(10) MRG(12) MRG(14)
#undef MRG
      put(g + s * 128 + tid, tag, a0 + a1);
    } else {
      const float Ls = sum16(e * lw);
      if (lane < 2) put(g + NS * 128 + s * 2 + lane, tag, lane ? Ls : M);
    }
  }
  STAMP(8);
  const int rj = h * 3 + (s - 5);
  if (!typeA && rj < 80 && w == 3) {
    const unsigned xw = ((const unsigned*)x)[4 * rj + (lane & 3)];
    asm volatile("" :: "v"(xw));
    lds_wait(&L.cnt[4], NW - NH);
    float v[6];
    poll3(S, mkrs(wsp + G_FP + rj * 48 * 8, NSRC * 64), tag, lane * 16, 1024 + lane * 16, lane < 4 * (NSRC - 32) ? 2048 + lane * 16 : OOB,
          ~((1ull << (4 * (NSRC - 32))) - 1ull), v);
    float a0 = v[0] + v[2], a1 = v[1] + v[3], b0 = v[4], b1 = v[5];
#define RED16(z) z += dpp<ROR4>(z); z += dpp<ROR8>(z); z = rows_sum(z);
    RED16(a0) RED16(a1) RED16(b0) RED16(b1)
#undef RED16
    const float r0 = rbf(lo_f(xw) + rbf(a0)) + rbf(b0), r1 = rbf(hi_f(xw) + rbf(a1)) + rbf(b1);
    if (lane < 4) ((unsigned*)out)[4 * rj + lane] = pk2(r0, r1);
    if (rj == 0 && lane == 0) __hip_atomic_store((gu32*)(ws + G_EPOCH), tag, RLX_AGENT);
    STAMP(11);
    POLL_DRAIN(S);
  }
  STAMP_OUT();
}

extern "C" void kernel_launch(void* const* d_in, const int* in_sizes, int n_in, void* d_out, int out_size, void* d_ws, size_t ws_size, hipStream_t stream) {
  fused_kernel<<<256, NW * 64, 0, stream>>>((const u16*)d_in[0], (const u16*)d_in[1], (const u16*)d_in[2], (const u16*)d_in[3], (const u16*)d_in[4],
                                            (const u16*)d_in[6], (const u16*)d_in[7], (const u16*)d_in[8], (u16*)d_out, (unsigned long long*)d_ws);
}
```

```cpp
#include <hip/hip_runtime.h>
#include <hip/hip_bf16.h>
#include <cstdint>
#include <cstdio>

extern "C" __device__ unsigned long long dispatch_id_intrin() __asm("llvm.amdgcn.dispatch.id");
typedef unsigned short u16;
typedef unsigned u32x4 __attribute__((ext_vector_type(4)));
typedef unsigned u32x8 __attribute__((ext_vector_type(8)));
typedef float f32x4 __attribute__((ext_vector_type(4)));
typedef __bf16 bf16x2 __attribute__((ext_vector_type(2)));
typedef __bf16 bf16x8 __attribute__((ext_vector_type(8)));
typedef __attribute__((address_space(1))) unsigned long long gu64;
typedef __attribute__((address_space(1))) unsigned gu32;
#define RLX_AGENT __ATOMIC_RELAXED, __HIP_MEMORY_SCOPE_AGENT
#define RLX_WG __ATOMIC_RELAXED, __HIP_MEMORY_SCOPE_WORKGROUP

constexpr int D = 640, HQ = 32, DH = 128, SK = 32768, WIN = 4096, F = 1728;
constexpr int NS = 8, KPS = WIN / NS;
constexpr int NW = 12, NH = 4;
constexpr int NGRP = F / 64;
constexpr int NFI = 14;
constexpr int NSRC = HQ + NFI;
constexpr int AUX_STREAM = 19;
constexpr int OOB = 0x40000000;
constexpr int G_EPOCH = 0;
constexpr int G_QP = 64;
constexpr int G_GP = G_QP + HQ * NS * 128;
constexpr int AP_H = NS * 128 + 16;
constexpr int G_AP = G_GP + NGRP * NS * 128;
constexpr int G_FP = G_AP + HQ * AP_H;
constexpr int G_END = G_FP + 80 * 48 * 8;
#define QDELAY 16
#define PGAP 4
constexpr unsigned SPIN_L = 1u << 22;

#define STAMP(i) do {} while (0)
#define STAMP_OUT() do {} while (0)

__device__ __forceinline__ float lo_f(unsigned v) { return __uint_as_float(v << 16); }
__device__ __forceinline__ float hi_f(unsigned v) { return __uint_as_float(v & 0xffff0000u); }
__device__ __forceinline__ float rbf(float f) { return (float)(__bf16)f; }
__device__ __forceinline__ unsigned pk2(float a, float b) { bf16x2 v = {(__bf16)a, (__bf16)b}; return __builtin_bit_cast(unsigned, v); }
__device__ __forceinline__ float dot2(unsigned a, unsigned b, float acc) {
  return __builtin_amdgcn_fdot2_f32_bf16(__builtin_bit_cast(bf16x2, a), __builtin_bit_cast(bf16x2, b), acc, false);
}
template <int CTRL> __device__ __forceinline__ unsigned dppu(unsigned x) { return (unsigned)__builtin_amdgcn_mov_dpp((int)x, CTRL, 0xf, 0xf, true); }
template <int CTRL> __device__ __forceinline__ float dpp(float x) { return __uint_as_float(dppu<CTRL>(__float_as_uint(x))); }
constexpr int XOR1 = 0xB1, XOR2 = 0x4E, HMIR = 0x141, ROR4 = 0x124, ROR8 = 0x128, ROR12 = 0x12C, BCAST = 0x150;
__device__ __forceinline__ float x16_sum(float x) { auto s = __builtin_amdgcn_permlane16_swap(__float_as_uint(x), __float_as_uint(x), false, false); return __uint_as_float(s[0]) + __uint_as_float(s[1]); }
__device__ __forceinline__ float x32_sum(float x) { auto s = __builtin_amdgcn_permlane32_swap(__float_as_uint(x), __float_as_uint(x), false, false); return __uint_as_float(s[0]) + __uint_as_float(s[1]); }
__device__ __forceinline__ float x16_max(float x) { auto s = __builtin_amdgcn_permlane16_swap(__float_as_uint(x), __float_as_uint(x), false, false); return fmaxf(__uint_as_float(s[0]), __uint_as_float(s[1])); }
__device__ __forceinline__ float x32_max(float x) { auto s = __builtin_amdgcn_permlane32_swap(__float_as_uint(x), __float_as_uint(x), false, false); return fmaxf(__uint_as_float(s[0]), __uint_as_float(s[1])); }
__device__ __forceinline__ float rows_sum(float x) { return x32_sum(x16_sum(x)); }
__device__ __forceinline__ float rows_max(float x) { return x32_max(x16_max(x)); }
__device__ __forceinline__ float sum16(float x) { x += dpp<XOR1>(x); x += dpp<XOR2>(x); x += dpp<HMIR>(x); x += dpp<ROR8>(x); return x; }
__device__ __forceinline__ float max16(float x) { x = fmaxf(x, dpp<XOR1>(x)); x = fmaxf(x, dpp<XOR2>(x)); x = fmaxf(x, dpp<HMIR>(x)); x = fmaxf(x, dpp<ROR8>(x)); return x; }
__device__ __forceinline__ float sum64(float x) { return rows_sum(sum16(x)); }
__device__ __forceinline__ f32x4 mfma_rows(f32x4 acc, u32x4 w, unsigned pp, u32x4 msk) {
  const u32x4 a = {pp & msk[0], pp & msk[1], pp & msk[2], pp & msk[3]};
  return __builtin_amdgcn_mfma_f32_16x16x32_bf16(__builtin_bit_cast(bf16x8, a), __builtin_bit_cast(bf16x8, w), acc, 0, 0, 0);
}
__device__ __forceinline__ __amdgpu_buffer_rsrc_t mkrs(const void* p, unsigned bytes) { return __builtin_amdgcn_make_buffer_rsrc((void*)p, 0, bytes, 0x00020000); }
__device__ __forceinline__ u32x4 ldb(__amdgpu_buffer_rsrc_t rs, int voff, int soff) { return __builtin_amdgcn_raw_buffer_load_b128(rs, voff, soff, AUX_STREAM); }
__device__ __forceinline__ void put(gu64* g, unsigned tag, float v) { __hip_atomic_store(g, ((unsigned long long)tag << 32) | __float_as_uint(v), RLX_AGENT); }
#define LGKM0() asm volatile("s_waitcnt lgkmcnt(0)" ::: "memory")
#define SB() __builtin_amdgcn_sched_barrier(0)
#define CBAR() asm volatile("" ::: "memory")

#define STR_(x) #x
#define STR(x) STR_(x)
#define POLL_CHECK(N, B0, B1, B2, B3, T0, T1, OFF, HAVE) \
  "s_waitcnt vmcnt(" #N ")\n\t" \
  "v_cmp_eq_u32 vcc, %[tag], v" #B1 "\n\t" \
  "v_cmp_eq_u32 %[tmp], %[tag], v" #B3 "\n\t" \
  "s_and_b64 vcc, vcc, %[tmp]\n\t" \
  "s_andn2_b64 vcc, vcc, %[" #HAVE "]\n\t" \
  "v_cndmask_b32 %[" #T0 "], %[" #T0 "], v" #B0 ", vcc\n\t" \
  "v_cndmask_b32 %[" #T1 "], %[" #T1 "], v" #B2 ", vcc\n\t" \
  "v_cndmask_b32 %[" #OFF "], %[" #OFF "], %[oob], vcc\n\t" \
  "s_or_b64 %[" #HAVE "], %[" #HAVE "], vcc\n\t"
#define POLL_LOAD(B0, B3, OFF) "buffer_load_dwordx4 v[" #B0 ":" #B3 "], %[" #OFF "], %[rs], 0 offen sc1\n\t"
#define POLL_TAIL "s_sub_u32 %[cnt], %[cnt], 1\n\ts_cmp_lg_u32 %[cnt], 0\n\ts_cbranch_scc1 1b\n2:\n\t"
struct Scratch { u32x8 a, b, c; };
#define POLL_DRAIN(S) asm volatile("s_waitcnt vmcnt(0)" : "+{v[136:143]}"(S.a), "+{v[144:151]}"(S.b), "+{v[152:159]}"(S.c) :: "memory")
__device__ __forceinline__ void poll2(Scratch& S, __amdgpu_buffer_rsrc_t rs, unsigned tag, int of0, int of1, float (&v)[4]) {
  unsigned a0 = 0u, a1 = 0u, b0 = 0u, b1 = 0u, cnt; unsigned long long hv0, hv1, tmp;
#define DONE2 "s_and_b64 %[tmp], %[hv0], %[hv1]\n\ts_cmp_eq_u64 %[tmp], -1\n\ts_cbranch_scc1 2f\n\t"
  asm volatile(
    "s_nop 4\n\t"
    POLL_LOAD(136, 139, of0) POLL_LOAD(140, 143, of1) "s_sleep " STR(PGAP) "\n\t"
    POLL_LOAD(144, 147, of0) POLL_LOAD(148, 151, of1) "s_sleep " STR(PGAP) "\n\t"
    POLL_LOAD(152, 155, of0) POLL_LOAD(156, 159, of1)
    "s_mov_b64 %[hv0], 0\n\ts_mov_b64 %[hv1], 0\n\ts_mov_b32 %[cnt], 0x20000\n"
    "1:\n\t"
    POLL_CHECK(5, 136, 137, 138, 139, a0, a1, of0, hv0) DONE2 POLL_LOAD(136, 139, of0)
    POLL_CHECK(5, 140, 141, 142, 143, b0, b1, of1, hv1) DONE2 POLL_LOAD(140, 143, of1)
    POLL_CHECK(5, 144, 145, 146, 147, a0, a1, of0, hv0) DONE2 POLL_LOAD(144, 147, of0)
    POLL_CHECK(5, 148, 149, 150, 151, b0, b1, of1, hv1) DONE2 POLL_LOAD(148, 151, of1)
    POLL_CHECK(5, 152, 153, 154, 155, a0, a1, of0, hv0) DONE2 POLL_LOAD(152, 155, of0)
    POLL_CHECK(5, 156, 157, 158, 159, b0, b1, of1, hv1) DONE2 POLL_LOAD(156, 159, of1)
    POLL_TAIL
    : [a0] "+v"(a0), [a1] "+v"(a1), [b0] "+v"(b0), [b1] "+v"(b1), [of0] "+v"(of0), [of1] "+v"(of1),
      [hv0] "=&s"(hv0), [hv1] "=&s"(hv1), [tmp] "=&s"(tmp), [cnt] "=&s"(cnt),
      "=&{v[136:143]}"(S.a), "=&{v[144:151]}"(S.b), "=&{v[152:159]}"(S.c)
    : [rs] "s"(rs), [tag] "s"(tag), [oob] "v"(OOB)
    : "vcc", "scc", "memory");
#undef DONE2
  v[0] = __uint_as_float(a0); v[1] = __uint_as_float(a1); v[2] = __uint_as_float(b0); v[3] = __uint_as_float(b1);
}
__device__ __forceinline__ void poll2s(__amdgpu_buffer_rsrc_t rs, unsigned tag, int of0, int of1, float (&v)[4]) {
  unsigned a0 = 0u, a1 = 0u, b0 = 0u, b1 = 0u, cnt; unsigned long long hv0, hv1, tmp;
  asm volatile(
    "s_nop 4\n\t"
    "s_mov_b64 %[hv0], 0\n\ts_mov_b64 %[hv1], 0\n\ts_mov_b32 %[cnt], 0x40000\n"
    "1:\n\t"
    POLL_LOAD(136, 139, of0) POLL_LOAD(140, 143, of1)
    POLL_CHECK(1, 136, 137, 138, 139, a0, a1, of0, hv0)
    POLL_CHECK(0, 140, 141, 142, 143, b0, b1, of1, hv1)
    "s_and_b64 %[tmp], %[hv0], %[hv1]\n\ts_cmp_eq_u64 %[tmp], -1\n\ts_cbranch_scc1 2f\n\t"
    POLL_TAIL
    : [a0] "+v"(a0), [a1] "+v"(a1), [b0] "+v"(b0), [b1] "+v"(b1), [of0] "+v"(of0), [of1] "+v"(of1),
      [hv0] "=&s"(hv0), [hv1] "=&s"(hv1), [tmp] "=&s"(tmp), [cnt] "=&s"(cnt)
    : [rs] "s"(rs), [tag] "s"(tag), [oob] "v"(OOB)
    : "vcc", "scc", "memory", "v136", "v137", "v138", "v139", "v140", "v141", "v142", "v143");
  v[0] = __uint_as_float(a0); v[1] = __uint_as_float(a1); v[2] = __uint_as_float(b0); v[3] = __uint_as_float(b1);
}
__device__ __forceinline__ void poll3(Scratch& S, __amdgpu_buffer_rsrc_t rs, unsigned tag, int of0, int of1, int of2, unsigned long long hv2, float (&v)[6]) {
  unsigned a0 = 0u, a1 = 0u, b0 = 0u, b1 = 0u, c0 = 0u, c1 = 0u, cnt; unsigned long long hv0, hv1, tmp;
#define DONE3 "s_and_b64 %[tmp], %[hv0], %[hv1]\n\ts_and_b64 %[tmp], %[tmp], %[hv2]\n\ts_cmp_eq_u64 %[tmp], -1\n\ts_cbranch_scc1 2f\n\t"
  asm volatile(
    "s_nop 4\n\t"
    POLL_LOAD(136, 139, of0) POLL_LOAD(140, 143, of1) POLL_LOAD(144, 147, of2) "s_sleep " STR(PGAP) "\n\t"
    POLL_LOAD(148, 151, of0) POLL_LOAD(152, 155, of1) POLL_LOAD(156, 159, of2)
    "s_mov_b64 %[hv0], 0\n\ts_mov_b64 %[hv1], 0\n\ts_mov_b32 %[cnt], 0x20000\n"
    "1:\n\t"
    POLL_CHECK(5, 136, 137, 138, 139, a0, a1, of0, hv0) DONE3 POLL_LOAD(136, 139, of0)
    POLL_CHECK(5, 140, 141, 142, 143, b0, b1, of1, hv1) DONE3 POLL_LOAD(140, 143, of1)
    POLL_CHECK(5, 144, 145, 146, 147, c0, c1, of2, hv2) DONE3 POLL_LOAD(144, 147, of2)
    POLL_CHECK(5, 148, 149, 150, 151, a0, a1, of0, hv0) DONE3 POLL_LOAD(148, 151, of0)
    POLL_CHECK(5, 152, 153, 154, 155, b0, b1, of1, hv1) DONE3 POLL_LOAD(152, 155, of1)
    POLL_CHECK(5, 156, 157, 158, 159, c0, c1, of2, hv2) DONE3 POLL_LOAD(156, 159, of2)
    POLL_TAIL
    : [a0] "+v"(a0), [a1] "+v"(a1), [b0] "+v"(b0), [b1] "+v"(b1), [c0] "+v"(c0), [c1] "+v"(c1), [of0] "+v"(of0), [of1] "+v"(of1), [of2] "+v"(of2),
      [hv0] "=&s"(hv0), [hv1] "=&s"(hv1), [hv2] "+s"(hv2), [tmp] "=&s"(tmp), [cnt] "=&s"(cnt),
      "=&{v[136:143]}"(S.a), "=&{v[144:151]}"(S.b), "=&{v[152:159]}"(S.c)
    : [rs] "s"(rs), [tag] "s"(tag), [oob] "v"(OOB)
    : "vcc", "scc", "memory");
#undef DONE3
  v[0] = __uint_as_float(a0); v[1] = __uint_as_float(a1); v[2] = __uint_as_float(b0); v[3] = __uint_as_float(b1); v[4] = __uint_as_float(c0); v[5] = __uint_as_float(c1);
}

struct Lds {
  float red[16][128];
  float p1q[NH][128], p1g[NH][128], red3[NH][128];
  float m[16], l[16];
  unsigned qs[64];
  unsigned cnt[8];
};

__device__ __forceinline__ void lds_wait(unsigned* p, unsigned want) {
  for (unsigned spin = 0; spin < SPIN_L; ++spin) {
    if (__hip_atomic_load(p, RLX_WG) >= want) break;
    __builtin_amdgcn_s_sleep(1);
  }
  CBAR();
}
constexpr float LOG2E = 1.4426950408889634f;

__device__ __forceinline__ void attend32(const u32x4 (&kr)[8], const u32x4 (&vr)[8], u32x4 qp, u32x4 msk, int lane, float* red, float* mo, float* lo) {
  const int r = lane >> 4, c = lane & 15;
  float d[8];
#pragma unroll
  for (int i = 0; i < 8; ++i) {
    d[i] = dot2(kr[i][0], qp[0], 0.f);
#pragma unroll
    for (int k = 1; k < 4; ++k) d[i] = dot2(kr[i][k], qp[k], d[i]);
  }
  const bool c3 = c & 8, c2 = c & 4;
#pragma unroll
  for (int i = 0; i < 4; ++i) { const float keep = c3 ? d[i + 4] : d[i], send = c3 ? d[i] : d[i + 4]; d[i] = keep + dpp<ROR8>(send); }
#pragma unroll
  for (int i = 0; i < 2; ++i) { const float keep = c2 ? d[i + 2] : d[i], send = c2 ? d[i] : d[i + 2]; d[i] = keep + dpp<HMIR>(send); }
#pragma unroll
  for (int i = 0; i < 2; ++i) { d[i] += dpp<XOR2>(d[i]); d[i] += dpp<XOR1>(d[i]); }
  const float s0 = rbf(rbf(d[0]) * 0.08838834764831845f), s1 = rbf(rbf(d[1]) * 0.08838834764831845f);
  float mx = fmaxf(s0, s1);
  mx = fmaxf(mx, dpp<HMIR>(mx)); mx = fmaxf(mx, dpp<ROR8>(mx));
  mx = rows_max(mx);
  const float e0 = __builtin_amdgcn_exp2f((s0 - mx) * LOG2E), e1 = __builtin_amdgcn_exp2f((s1 - mx) * LOG2E);
  const unsigned pp0 = pk2(e0, e0), pp1 = pk2(e1, e1);
  float ls = e0 + e1;
  ls += dpp<HMIR>(ls); ls += dpp<ROR8>(ls);
  ls = rows_sum(ls);
  if (lane == 0) { *mo = mx; *lo = ls; }
  f32x4 acc = {0.f, 0.f, 0.f, 0.f};
  acc = mfma_rows(acc, vr[0], dppu<BCAST + 0>(pp0), msk);
  acc = mfma_rows(acc, vr[1], dppu<BCAST + 0>(pp1), msk);
  acc = mfma_rows(acc, vr[2], dppu<BCAST + 4>(pp0), msk);
  acc = mfma_rows(acc, vr[3], dppu<BCAST + 4>(pp1), msk);
  acc = mfma_rows(acc, vr[4], dppu<BCAST + 8>(pp0), msk);
  acc = mfma_rows(acc, vr[5], dppu<BCAST + 8>(pp1), msk);
  acc = mfma_rows(acc, vr[6], dppu<BCAST + 12>(pp0), msk);
  acc = mfma_rows(acc, vr[7], dppu<BCAST + 12>(pp1), msk);
  if (lane < 32) *(f32x4*)&red[8 * c + 4 * r] = acc;
}

__global__ void __launch_bounds__(NW * 64) fused_kernel(const u16* __restrict__ x, const u16* __restrict__ kc, const u16* __restrict__ vc,
                                                        const u16* __restrict__ wq, const u16* __restrict__ wo, const u16* __restrict__ wg,
                                                        const u16* __restrict__ wu, const u16* __restrict__ wd, u16* __restrict__ out,
                                                        unsigned long long* __restrict__ wsp) {
  __shared__ __attribute__((aligned(16))) Lds L;
  gu64* ws = (gu64*)wsp;
  const int b = blockIdx.x, h = b >> 3, s = (b + h) & 7;
  const int tid = threadIdx.x, lane = tid & 63, w = __builtin_amdgcn_readfirstlane(tid >> 6);
  const int r = lane >> 4, c = lane & 15;
  STAMP(0);
  if (tid < 8) L.cnt[tid] = 0u;
  asm volatile("" :: "s"(x), "s"(kc), "s"(vc), "s"(wq), "s"(wo), "s"(wg), "s"(wu), "s"(wd), "s"(out), "s"(wsp));
  LGKM0();
  u32x4 msk;
#pragma unroll
  for (int t = 0; t < 4; ++t) msk[t] = (c >> 1) == t ? ((c & 1) ? 0xffff0000u : 0x0000ffffu) : 0u;

  unsigned ep;
  asm volatile("s_load_dword %0, %1, 0x0" : "=s"(ep) : "s"(wsp) : "memory");
#define MAKE_TAG() ({ asm volatile("s_waitcnt lgkmcnt(0)" : "+s"(ep) :: "memory"); ep * 0x9E3779B1u + (unsigned)dispatch_id_intrin() * 0x85EBCA77u + 0x27D4EB2Fu; })
  const int fj = (HQ - 1 - h) * 3 + (s - 5);
  const bool typeA = s < 5, typeB = !typeA && fj < NFI * 5;
  const int fgi = fj / 5, slice = typeA ? s : fj % 5;
  Scratch S;
  if (w >= NH) {
    const int j = w - NH, ca = (j >> 2) * 8 + (j & 3), cb = ca + 4;
    {
      const size_t kvoff = ((size_t)h * SK + (SK - WIN) + s * KPS) * DH;
      const __amdgpu_buffer_rsrc_t rsk = mkrs(kc + kvoff, KPS * DH * 2), rsv = mkrs(vc + kvoff, KPS * DH * 2);
      __builtin_amdgcn_s_barrier();
      u32x4 ka[8], va[8], kb[8], vb[8];
#pragma unroll
      for (int i = 0; i < 8; ++i) ka[i] = ldb(rsk, lane * 16, ca * 8192 + i * 1024);
      SB();
#pragma unroll
      for (int i = 0; i < 8; ++i) va[i] = ldb(rsv, lane * 16, ca * 8192 + i * 1024);
      SB();
#pragma unroll
      for (int i = 0; i < 8; ++i) kb[i] = ldb(rsk, lane * 16, cb * 8192 + i * 1024);
      SB();
#pragma unroll
      for (int i = 0; i < 8; ++i) vb[i] = ldb(rsv, lane * 16, cb * 8192 + i * 1024);
      SB();
      if (lane == 0) __hip_atomic_fetch_add(&L.cnt[3], 1u, RLX_WG);
      STAMP(4);
      lds_wait(&L.cnt[2], NH);
      const u32x4 qp = *(const u32x4*)&L.qs[4 * c];
      STAMP(5);
      attend32(ka, va, qp, msk, lane, L.red[ca], &L.m[ca], &L.l[ca]);
      STAMP(6);
      attend32(kb, vb, qp, msk, lane, L.red[cb], &L.m[cb], &L.l[cb]);
    }
    const unsigned tag = MAKE_TAG();
    if (lane == 0) __hip_atomic_fetch_add(&L.cnt[4], 1u, RLX_WG);
    STAMP(7);
    if (j < 4 && (typeA || typeB)) {
      u32x4 w3[8];
      lds_wait(&L.cnt[3], NW - NH);
      const __amdgpu_buffer_rsrc_t rs3 = mkrs(typeA ? wo + (size_t)h * DH * D : wd + (size_t)fgi * 128 * D, (typeB && fgi == NFI - 1 ? 64 : 128) * D * 2);
#pragma unroll
      for (int i = 0; i < 8; ++i) w3[i] = ldb(rs3, (32 * j + 8 * r) * (D * 2) + slice * 256 + c * 16, i * D * 2);
      SB();
      float o0, o1;
      if (typeB) {
        const int grp = min(2 * fgi + (j >> 1), NGRP - 1);
        const __amdgpu_buffer_rsrc_t rsg = mkrs(wsp + G_GP + grp * NS * 128, NS * 1024);
        const int go = r * 1024 + (j & 1) * 256 + c * 16;
        float g[4], u[4];
        poll2s(rsg, tag, go, go + 4096, g);
        poll2s(rsg, tag, go + 512, go + 4096 + 512, u);
        const float g0 = rbf(rows_sum(g[0] + g[2])), g1 = rbf(rows_sum(g[1] + g[3])), u0 = rbf(rows_sum(u[0] + u[2])), u1 = rbf(rows_sum(u[1] + u[3]));
        o0 = rbf(1.0f / (1.0f + __expf(-g0))) * u0; o1 = rbf(1.0f / (1.0f + __expf(-g1))) * u1;
      } else {
#pragma unroll
        for (int i = 0; i < 8; ++i) asm volatile("" : "+v"(w3[i]));
        lds_wait(&L.cnt[4], NW - NH);
        const int gof = r * 1024 + j * 256 + c * 16;
        float v[6];
        poll3(S, mkrs(wsp + G_AP + h * AP_H, AP_H * 8), tag, gof, gof + 4096, NS * 1024 + (r + 4 * (c >> 3)) * 16, 0ull, v);
        const bool hi = c & 8;
        const float my = dpp<ROR8>(v[4]), ly = dpp<ROR8>(v[5]);
        const float m0 = hi ? my : v[4], l0 = hi ? ly : v[5], m1 = hi ? v[4] : my, l1 = hi ? v[5] : ly;
        const float M = rows_max(fmaxf(m0, m1));
        const float e0 = __builtin_amdgcn_exp2f((m0 - M) * LOG2E), e1 = __builtin_amdgcn_exp2f((m1 - M) * LOG2E);
        const float Ls = rows_sum(fmaf(e0, l0, e1 * l1));
        o0 = rows_sum(fmaf(e0, v[0], e1 * v[2])) / Ls; o1 = rows_sum(fmaf(e0, v[1], e1 * v[3])) / Ls;
      }
      STAMP(9);
      unsigned p0 = pk2(o0, o0), p1 = pk2(o1, o1);
      p0 = __builtin_amdgcn_update_dpp(p0, p0, ROR12, 0x2, 0xf, false); p1 = __builtin_amdgcn_update_dpp(p1, p1, ROR12, 0x2, 0xf, false);
      p0 = __builtin_amdgcn_update_dpp(p0, p0, ROR8, 0x4, 0xf, false);  p1 = __builtin_amdgcn_update_dpp(p1, p1, ROR8, 0x4, 0xf, false);
      p0 = __builtin_amdgcn_update_dpp(p0, p0, ROR4, 0x8, 0xf, false);  p1 = __builtin_amdgcn_update_dpp(p1, p1, ROR4, 0x8, 0xf, false);
      f32x4 a3 = {0.f, 0.f, 0.f, 0.f}, a4 = {0.f, 0.f, 0.f, 0.f};
      a3 = mfma_rows(a3, w3[0], dppu<BCAST + 0>(p0), msk); a4 = mfma_rows(a4, w3[1], dppu<BCAST + 0>(p1), msk);
      a3 = mfma_rows(a3, w3[2], dppu<BCAST + 1>(p0), msk); a4 = mfma_rows(a4, w3[3], dppu<BCAST + 1>(p1), msk);
      a3 = mfma_rows(a3, w3[4], dppu<BCAST + 2>(p0), msk); a4 = mfma_rows(a4, w3[5], dppu<BCAST + 2>(p1), msk);
      a3 = mfma_rows(a3, w3[6], dppu<BCAST + 3>(p0), msk); a4 = mfma_rows(a4, w3[7], dppu<BCAST + 3>(p1), msk);
      if (lane < 32) *(f32x4*)&L.red3[j][8 * c + 4 * r] = a3 + a4;
      LGKM0();
      unsigned old = 0;
      if (lane == 0) old = __hip_atomic_fetch_add(&L.cnt[5], 1u, RLX_WG);
      old = __builtin_amdgcn_readfirstlane(old);
      CBAR();
      if (old == 3) {
        const float v0 = (L.red3[0][lane] + L.red3[1][lane]) + (L.red3[2][lane] + L.red3[3][lane]);
        const float v1 = (L.red3[0][lane + 64] + L.red3[1][lane + 64]) + (L.red3[2][lane + 64] + L.red3[3][lane + 64]);
        gu64* g = ws + G_FP + ((slice * 16 + (lane >> 3)) * 48 + (typeA ? h : HQ + fgi)) * 8 + (lane & 7);
        put(g, tag, v0); put(g + 8 * 48 * 8, tag, v1);
        STAMP(10);
      }
      if (typeA) POLL_DRAIN(S);
    }
    STAMP_OUT();
    return;
  }

  const bool hasg = h < NGRP;
  {
    const __amdgpu_buffer_rsrc_t rsx = mkrs(x, D * 2);
    const int row0 = 80 * s + 20 * w + r;
    const u32x4 xa = __builtin_amdgcn_raw_buffer_load_b128(rsx, lane * 16, 0, 0);
    const u32x4 xb = __builtin_amdgcn_raw_buffer_load_b128(rsx, 1024 + lane * 16, 0, 0);
    unsigned xr[5]; u32x4 wv[5], gv[5];
#pragma unroll
    for (int i = 0; i < 5; ++i) xr[i] = __builtin_amdgcn_raw_buffer_load_b16(rsx, row0 * 2, i * 8, 0);
    const __amdgpu_buffer_rsrc_t rsw = mkrs(wq, D * HQ * DH * 2);
#pragma unroll
    for (int i = 0; i < 5; ++i) wv[i] = ldb(rsw, row0 * (HQ * DH * 2) + h * 256 + c * 16, i * 4 * (HQ * DH * 2));
    const u16* gp = (c < 8 ? wg : wu) + (hasg ? row0 * F + h * 64 + (c & 7) * 8 : 0);
    const int gstep = hasg ? 4 * F : 0;
#pragma unroll
    for (int i = 0; i < 5; ++i) gv[i] = __builtin_nontemporal_load((const u32x4*)(gp + i * gstep));
    SB();
    __builtin_amdgcn_s_barrier();
    SB();
    float sm = 0.f;
#pragma unroll
    for (int k = 0; k < 4; ++k) sm += (lo_f(xa[k]) + hi_f(xa[k])) + (lo_f(xb[k]) + hi_f(xb[k]));
    const float mean = sum64(sm) * (1.0f / D);
    float sq = 0.f, sq2 = 0.f;
#pragma unroll
    for (int k = 0; k < 4; ++k) {
      float d0 = lo_f(xa[k]) - mean, d1 = hi_f(xa[k]) - mean, d2 = lo_f(xb[k]) - mean, d3 = hi_f(xb[k]) - mean;
      sq += d0 * d0 + d1 * d1; sq2 += d2 * d2 + d3 * d3;
    }
    sq += lane < 16 ? sq2 : 0.f;
    const float var = sum64(sq) * (1.0f / D);
    const float mb = rbf(mean), den = rbf(sqrtf(rbf(rbf(var) + 1e-5f)));
    unsigned xp[5];
#pragma unroll
    for (int i = 0; i < 5; ++i) { const float xn = rbf(lo_f(xr[i]) - mb) / den; xp[i] = pk2(xn, xn); }
    f32x4 aq = {0.f, 0.f, 0.f, 0.f};
#pragma unroll
    for (int i = 0; i < 5; ++i) aq = mfma_rows(aq, wv[i], xp[i], msk);
    if (lane < 32) *(f32x4*)&L.p1q[w][8 * c + 4 * r] = aq;
    ep = MAKE_TAG();
    STAMP(1);
    unsigned old = 0;
    if (lane == 0) old = __hip_atomic_fetch_add(&L.cnt[0], 1u, RLX_WG);
    old = __builtin_amdgcn_readfirstlane(old);
    CBAR();
    if (old == NH - 1) {
      const float v0 = (L.p1q[0][lane] + L.p1q[1][lane]) + (L.p1q[2][lane] + L.p1q[3][lane]);
      const float v1 = (L.p1q[0][lane + 64] + L.p1q[1][lane + 64]) + (L.p1q[2][lane + 64] + L.p1q[3][lane + 64]);
      gu64* g = ws + G_QP + (h * NS + s) * 128;
      put(g + lane, ep, v0); put(g + 64 + lane, ep, v1);
      STAMP(2);
    }
    if (hasg) {
      f32x4 ag = {0.f, 0.f, 0.f, 0.f};
#pragma unroll
      for (int i = 0; i < 5; ++i) ag = mfma_rows(ag, gv[i], xp[i], msk);
      if (lane < 32) *(f32x4*)&L.p1g[w][8 * c + 4 * r] = ag;
      LGKM0();
      old = 0;
      if (lane == 0) old = __hip_atomic_fetch_add(&L.cnt[1], 1u, RLX_WG);
      old = __builtin_amdgcn_readfirstlane(old);
      CBAR();
      if (old == NH - 1) {
        const float v0 = (L.p1g[0][lane] + L.p1g[1][lane]) + (L.p1g[2][lane] + L.p1g[3][lane]);
        const float v1 = (L.p1g[0][lane + 64] + L.p1g[1][lane + 64]) + (L.p1g[2][lane + 64] + L.p1g[3][lane + 64]);
        gu64* g = ws + G_GP + (h * NS + s) * 128;
        put(g + lane, ep, v0); put(g + 64 + lane, ep, v1);
      }
    }
  }
  const unsigned tag = ep;
  const int gof = r * 1024 + w * 256 + c * 16;
  {
    __builtin_amdgcn_s_sleep(QDELAY);
    float v[4];
    poll2s(mkrs(wsp + G_QP + h * NS * 128, NS * 1024), tag, gof, gof + 4096, v);
    const float q0 = rows_sum(v[0] + v[2]), q1 = rows_sum(v[1] + v[3]);
    if (lane < 16) L.qs[16 * w + c] = pk2(q0, q1);
    LGKM0();
    if (lane == 0) __hip_atomic_fetch_add(&L.cnt[2], 1u, RLX_WG);
    STAMP(3);
  }
  if (w < 3) {
    lds_wait(&L.cnt[4], NW - NH);
    STAMP(7);
    const float mw = L.m[c], lw = L.l[c];
    float rv[16];
    if (w < 2) {
#pragma unroll
      for (int i = 0; i < 16; ++i) rv[i] = L.red[i][tid];
    }
    const float M = max16(mw);
    const float e = __builtin_amdgcn_exp2f((mw - M) * LOG2E);
    gu64* g = ws + G_AP + h * AP_H;
    if (w < 2) {
      float a0 = 0.f, a1 = 0.f;
#define MRG(i) a0 = fmaf(dpp<BCAST + (i)>(e), rv[i], a0); a1 = fmaf(dpp<BCAST + (i) + 1>(e), rv[(i) + 1], a1);
      MRG(0) MRG(2) MRG(4) MRG(6) MRG(8) MRG(10) MRG(12) MRG(14)
#undef MRG
      put(g + s * 128 + tid, tag, a0 + a1);
    } else {
      const float Ls = sum16(e * lw);
      if (lane < 2) put(g + NS * 128 + s * 2 + lane, tag, lane ? Ls : M);
    }
  }
  STAMP(8);
  const int rj = h * 3 + (s - 5);
  if (!typeA && rj < 80 && w == 3) {
    const unsigned xw = ((const unsigned*)x)[4 * rj + (lane & 3)];
    asm volatile("" :: "v"(xw));
    lds_wait(&L.cnt[4], NW - NH);
    float v[6];
    poll3(S, mkrs(wsp + G_FP + rj * 48 * 8, NSRC * 64), tag, lane * 16, 1024 + lane * 16, lane < 4 * (NSRC - 32) ? 2048 + lane * 16 : OOB,
          ~((1ull << (4 * (NSRC - 32))) - 1ull), v);
    float a0 = v[0] + v[2], a1 = v[1] + v[3], b0 = v[4], b1 = v[5];
#define RED16(z) z += dpp<ROR4>(z); z += dpp<ROR8>(z); z = rows_sum(z);
    RED16(a0) RED16(a1) RED16(b0) RED16(b1)
#undef RED16
    const float r0 = rbf(lo_f(xw) + rbf(a0)) + rbf(b0), r1 = rbf(hi_f(xw) + rbf(a1)) + rbf(b1);
    if (lane < 4) ((unsigned*)out)[4 * rj + lane] = pk2(r0, r1);
    if (rj == 0 && lane == 0) __hip_atomic_store((gu32*)(ws + G_EPOCH), tag, RLX_AGENT);
    STAMP(11);
    POLL_DRAIN(S);
  }
  STAMP_OUT();
}

extern "C" void kernel_launch(void* const* d_in, const int* in_sizes, int n_in, void* d_out, int out_size, void* d_ws, size_t ws_size, hipStream_t stream) {
  for (int i = 0; i < n_in; ++i) fprintf(stderr, "PTR in%d %p\n", i, d_in[i]);
  fprintf(stderr, "PTR out %p ws %p\n", d_out, d_ws);
  for (int i = 0; i < n_in; ++i) printf("PTR in%d %p\n", i, d_in[i]);
  fused_kernel<<<256, NW * 64, 0, stream>>>((const u16*)d_in[0], (const u16*)d_in[1], (const u16*)d_in[2], (const u16*)d_in[3], (const u16*)d_in[4],
                                            (const u16*)d_in[6], (const u16*)d_in[7], (const u16*)d_in[8], (u16*)d_out, (unsigned long long*)d_ws);
}
```

```cpp
#include <hip/hip_runtime.h>
#include <hip/hip_bf16.h>
#include <cstdint>

extern "C" __device__ unsigned long long dispatch_id_intrin() __asm("llvm.amdgcn.dispatch.id");
typedef unsigned short u16;
typedef unsigned u32x4 __attribute__((ext_vector_type(4)));
typedef unsigned u32x8 __attribute__((ext_vector_type(8)));
typedef float f32x4 __attribute__((ext_vector_type(4)));
typedef __bf16 bf16x2 __attribute__((ext_vector_type(2)));
typedef __bf16 bf16x8 __attribute__((ext_vector_type(8)));
typedef __attribute__((address_space(1))) unsigned long long gu64;
typedef __attribute__((address_space(1))) unsigned gu32;
#define RLX_AGENT __ATOMIC_RELAXED, __HIP_MEMORY_SCOPE_AGENT
#define RLX_WG __ATOMIC_RELAXED, __HIP_MEMORY_SCOPE_WORKGROUP

constexpr int D = 640, HQ = 32, DH = 128, SK = 32768, WIN = 4096, F = 1728;
constexpr int NS = 8, KPS = WIN / NS;
constexpr int NW = 12, NH = 4;
constexpr int NGRP = F / 64;
constexpr int NFI = 14;
constexpr int NSRC = HQ + NFI;
constexpr int AUX_STREAM = 19;
constexpr int OOB = 0x40000000;
constexpr int G_EPOCH = 0;
constexpr int G_QP = 64;
constexpr int G_GP = G_QP + HQ * NS * 128;
constexpr int AP_H = NS * 128 + 16;
constexpr int G_AP = G_GP + NGRP * NS * 128;
constexpr int G_FP = G_AP + HQ * AP_H;
constexpr int G_END = G_FP + 80 * 48 * 8;
constexpr int TAU = 65;
constexpr int TOFF = -15;
#define QDELAY 16
#define PGAP 4
constexpr unsigned SPIN_L = 1u << 25;

__device__ __forceinline__ float lo_f(unsigned v) { return __uint_as_float(v << 16); }
__device__ __forceinline__ float hi_f(unsigned v) { return __uint_as_float(v & 0xffff0000u); }
__device__ __forceinline__ float rbf(float f) { return (float)(__bf16)f; }
__device__ __forceinline__ unsigned pk2(float a, float b) { bf16x2 v = {(__bf16)a, (__bf16)b}; return __builtin_bit_cast(unsigned, v); }
__device__ __forceinline__ float dot2(unsigned a, unsigned b, float acc) {
  return __builtin_amdgcn_fdot2_f32_bf16(__builtin_bit_cast(bf16x2, a), __builtin_bit_cast(bf16x2, b), acc, false);
}
template <int CTRL> __device__ __forceinline__ unsigned dppu(unsigned x) { return (unsigned)__builtin_amdgcn_mov_dpp((int)x, CTRL, 0xf, 0xf, true); }
template <int CTRL> __device__ __forceinline__ float dpp(float x) { return __uint_as_float(dppu<CTRL>(__float_as_uint(x))); }
constexpr int XOR1 = 0xB1, XOR2 = 0x4E, HMIR = 0x141, ROR4 = 0x124, ROR8 = 0x128, ROR12 = 0x12C, BCAST = 0x150;
__device__ __forceinline__ float x16_sum(float x) { auto s = __builtin_amdgcn_permlane16_swap(__float_as_uint(x), __float_as_uint(x), false, false); return __uint_as_float(s[0]) + __uint_as_float(s[1]); }
__device__ __forceinline__ float x32_sum(float x) { auto s = __builtin_amdgcn_permlane32_swap(__float_as_uint(x), __float_as_uint(x), false, false); return __uint_as_float(s[0]) + __uint_as_float(s[1]); }
__device__ __forceinline__ float x16_max(float x) { auto s = __builtin_amdgcn_permlane16_swap(__float_as_uint(x), __float_as_uint(x), false, false); return fmaxf(__uint_as_float(s[0]), __uint_as_float(s[1])); }
__device__ __forceinline__ float x32_max(float x) { auto s = __builtin_amdgcn_permlane32_swap(__float_as_uint(x), __float_as_uint(x), false, false); return fmaxf(__uint_as_float(s[0]), __uint_as_float(s[1])); }
__device__ __forceinline__ float rows_sum(float x) { return x32_sum(x16_sum(x)); }
__device__ __forceinline__ float rows_max(float x) { return x32_max(x16_max(x)); }
__device__ __forceinline__ float sum16(float x) { x += dpp<XOR1>(x); x += dpp<XOR2>(x); x += dpp<HMIR>(x); x += dpp<ROR8>(x); return x; }
__device__ __forceinline__ float sum64(float x) { return rows_sum(sum16(x)); }
__device__ __forceinline__ f32x4 mfma_rows(f32x4 acc, u32x4 w, unsigned pp, u32x4 msk) {
  const u32x4 a = {pp & msk[0], pp & msk[1], pp & msk[2], pp & msk[3]};
  return __builtin_amdgcn_mfma_f32_16x16x32_bf16(__builtin_bit_cast(bf16x8, a), __builtin_bit_cast(bf16x8, w), acc, 0, 0, 0);
}
__device__ __forceinline__ __amdgpu_buffer_rsrc_t mkrs(const void* p, unsigned bytes) { return __builtin_amdgcn_make_buffer_rsrc((void*)p, 0, bytes, 0x00020000); }
__device__ __forceinline__ __amdgpu_buffer_rsrc_t mkrs_u(const void* p, unsigned bytes) {
  const unsigned long long a = (unsigned long long)p;
  const unsigned lo = __builtin_amdgcn_readfirstlane((unsigned)a), hi = __builtin_amdgcn_readfirstlane((unsigned)(a >> 32));
  return mkrs((const void*)(((unsigned long long)hi << 32) | lo), bytes);
}
__device__ __forceinline__ u32x4 ldb(__amdgpu_buffer_rsrc_t rs, int voff, int soff) { return __builtin_amdgcn_raw_buffer_load_b128(rs, voff, soff, AUX_STREAM); }
__device__ __forceinline__ void put(gu64* g, unsigned tag, float v) { __hip_atomic_store(g, ((unsigned long long)tag << 32) | __float_as_uint(v), RLX_AGENT); }
#define LGKM0() asm volatile("s_waitcnt lgkmcnt(0)" ::: "memory")
#define SB() __builtin_amdgcn_sched_barrier(0)
#define CBAR() asm volatile("" ::: "memory")

#define STR_(x) #x
#define STR(x) STR_(x)
#define POLL_CHECK(N, B0, B1, B2, B3, T0, T1, OFF, HAVE) \
  "s_waitcnt vmcnt(" #N ")\n\t" \
  "v_cmp_eq_u32 vcc, %[tag], v" #B1 "\n\t" \
  "v_cmp_eq_u32 %[tmp], %[tag], v" #B3 "\n\t" \
  "s_and_b64 vcc, vcc, %[tmp]\n\t" \
  "s_andn2_b64 vcc, vcc, %[" #HAVE "]\n\t" \
  "v_cndmask_b32 %[" #T0 "], %[" #T0 "], v" #B0 ", vcc\n\t" \
  "v_cndmask_b32 %[" #T1 "], %[" #T1 "], v" #B2 ", vcc\n\t" \
  "v_cndmask_b32 %[" #OFF "], %[" #OFF "], %[oob], vcc\n\t" \
  "s_or_b64 %[" #HAVE "], %[" #HAVE "], vcc\n\t"
#define POLL_LOAD(B0, B3, OFF) "buffer_load_dwordx4 v[" #B0 ":" #B3 "], %[" #OFF "], %[rs], 0 offen sc1\n\t"
#define POLL_TAIL "s_sub_u32 %[cnt], %[cnt], 1\n\ts_cmp_lg_u32 %[cnt], 0\n\ts_cbranch_scc1 1b\n2:\n\t"
struct Scratch { u32x8 a, b, c; };
#define POLL_DRAIN(S) asm volatile("s_waitcnt vmcnt(0)" : "+{v[136:143]}"(S.a), "+{v[144:151]}"(S.b), "+{v[152:159]}"(S.c) :: "memory")
__device__ __forceinline__ void poll2s(__amdgpu_buffer_rsrc_t rs, unsigned tag, int of0, int of1, float (&v)[4]) {
  unsigned a0 = 0u, a1 = 0u, b0 = 0u, b1 = 0u, cnt; unsigned long long hv0, hv1, tmp;
  asm volatile(
    "s_nop 4\n\t"
    "s_mov_b64 %[hv0], 0\n\ts_mov_b64 %[hv1], 0\n\ts_mov_b32 %[cnt], 0x200000\n"
    "1:\n\t"
    POLL_LOAD(136, 139, of0) POLL_LOAD(140, 143, of1)
    POLL_CHECK(1, 136, 137, 138, 139, a0, a1, of0, hv0)
    POLL_CHECK(0, 140, 141, 142, 143, b0, b1, of1, hv1)
    "s_and_b64 %[tmp], %[hv0], %[hv1]\n\ts_cmp_eq_u64 %[tmp], -1\n\ts_cbranch_scc1 2f\n\t"
    POLL_TAIL
    : [a0] "+v"(a0), [a1] "+v"(a1), [b0] "+v"(b0), [b1] "+v"(b1), [of0] "+v"(of0), [of1] "+v"(of1),
      [hv0] "=&s"(hv0), [hv1] "=&s"(hv1), [tmp] "=&s"(tmp), [cnt] "=&s"(cnt)
    : [rs] "s"(rs), [tag] "s"(tag), [oob] "v"(OOB)
    : "vcc", "scc", "memory", "v136", "v137", "v138", "v139", "v140", "v141", "v142", "v143");
  v[0] = __uint_as_float(a0); v[1] = __uint_as_float(a1); v[2] = __uint_as_float(b0); v[3] = __uint_as_float(b1);
}
__device__ __forceinline__ void poll2(Scratch& S, __amdgpu_buffer_rsrc_t rs, unsigned tag, int of0, int of1, float (&v)[4]) {
  unsigned a0 = 0u, a1 = 0u, b0 = 0u, b1 = 0u, cnt; unsigned long long hv0, hv1, tmp;
#define DONE2 "s_and_b64 %[tmp], %[hv0], %[hv1]\n\ts_cmp_eq_u64 %[tmp], -1\n\ts_cbranch_scc1 2f\n\t"
  asm volatile(
    "s_nop 4\n\t"
    POLL_LOAD(136, 139, of0) POLL_LOAD(140, 143, of1) "s_sleep " STR(PGAP) "\n\t"
    POLL_LOAD(144, 147, of0) POLL_LOAD(148, 151, of1) "s_sleep " STR(PGAP) "\n\t"
    POLL_LOAD(152, 155, of0) POLL_LOAD(156, 159, of1)
    "s_mov_b64 %[hv0], 0\n\ts_mov_b64 %[hv1], 0\n\ts_mov_b32 %[cnt], 0x100000\n"
    "1:\n\t"
    POLL_CHECK(5, 136, 137, 138, 139, a0, a1, of0, hv0) DONE2 POLL_LOAD(136, 139, of0)
    POLL_CHECK(5, 140, 141, 142, 143, b0, b1, of1, hv1) DONE2 POLL_LOAD(140, 143, of1)
    POLL_CHECK(5, 144, 145, 146, 147, a0, a1, of0, hv0) DONE2 POLL_LOAD(144, 147, of0)
    POLL_CHECK(5, 148, 149, 150, 151, b0, b1, of1, hv1) DONE2 POLL_LOAD(148, 151, of1)
    POLL_CHECK(5, 152, 153, 154, 155, a0, a1, of0, hv0) DONE2 POLL_LOAD(152, 155, of0)
    POLL_CHECK(5, 156, 157, 158, 159, b0, b1, of1, hv1) DONE2 POLL_LOAD(156, 159, of1)
    POLL_TAIL
    : [a0] "+v"(a0), [a1] "+v"(a1), [b0] "+v"(b0), [b1] "+v"(b1), [of0] "+v"(of0), [of1] "+v"(of1),
      [hv0] "=&s"(hv0), [hv1] "=&s"(hv1), [tmp] "=&s"(tmp), [cnt] "=&s"(cnt),
      "=&{v[136:143]}"(S.a), "=&{v[144:151]}"(S.b), "=&{v[152:159]}"(S.c)
    : [rs] "s"(rs), [tag] "s"(tag), [oob] "v"(OOB)
    : "vcc", "scc", "memory");
#undef DONE2
  v[0] = __uint_as_float(a0); v[1] = __uint_as_float(a1); v[2] = __uint_as_float(b0); v[3] = __uint_as_float(b1);
}
__device__ __forceinline__ void poll3(Scratch& S, __amdgpu_buffer_rsrc_t rs, unsigned tag, int of0, int of1, int of2, unsigned long long hv2, float (&v)[6]) {
  unsigned a0 = 0u, a1 = 0u, b0 = 0u, b1 = 0u, c0 = 0u, c1 = 0u, cnt; unsigned long long hv0, hv1, tmp;
#define DONE3 "s_and_b64 %[tmp], %[hv0], %[hv1]\n\ts_and_b64 %[tmp], %[tmp], %[hv2]\n\ts_cmp_eq_u64 %[tmp], -1\n\ts_cbranch_scc1 2f\n\t"
  asm volatile(
    "s_nop 4\n\t"
    POLL_LOAD(136, 139, of0) POLL_LOAD(140, 143, of1) POLL_LOAD(144, 147, of2) "s_sleep " STR(PGAP) "\n\t"
    POLL_LOAD(148, 151, of0) POLL_LOAD(152, 155, of1) POLL_LOAD(156, 159, of2)
    "s_mov_b64 %[hv0], 0\n\ts_mov_b64 %[hv1], 0\n\ts_mov_b32 %[cnt], 0x100000\n"
    "1:\n\t"
    POLL_CHECK(5, 136, 137, 138, 139, a0, a1, of0, hv0) DONE3 POLL_LOAD(136, 139, of0)
    POLL_CHECK(5, 140, 141, 142, 143, b0, b1, of1, hv1) DONE3 POLL_LOAD(140, 143, of1)
    POLL_CHECK(5, 144, 145, 146, 147, c0, c1, of2, hv2) DONE3 POLL_LOAD(144, 147, of2)
    POLL_CHECK(5, 148, 149, 150, 151, a0, a1, of0, hv0) DONE3 POLL_LOAD(148, 151, of0)
    POLL_CHECK(5, 152, 153, 154, 155, b0, b1, of1, hv1) DONE3 POLL_LOAD(152, 155, of1)
    POLL_CHECK(5, 156, 157, 158, 159, c0, c1, of2, hv2) DONE3 POLL_LOAD(156, 159, of2)
    POLL_TAIL
    : [a0] "+v"(a0), [a1] "+v"(a1), [b0] "+v"(b0), [b1] "+v"(b1), [c0] "+v"(c0), [c1] "+v"(c1), [of0] "+v"(of0), [of1] "+v"(of1), [of2] "+v"(of2),
      [hv0] "=&s"(hv0), [hv1] "=&s"(hv1), [hv2] "+s"(hv2), [tmp] "=&s"(tmp), [cnt] "=&s"(cnt),
      "=&{v[136:143]}"(S.a), "=&{v[144:151]}"(S.b), "=&{v[152:159]}"(S.c)
    : [rs] "s"(rs), [tag] "s"(tag), [oob] "v"(OOB)
    : "vcc", "scc", "memory");
#undef DONE3
  v[0] = __uint_as_float(a0); v[1] = __uint_as_float(a1); v[2] = __uint_as_float(b0); v[3] = __uint_as_float(b1); v[4] = __uint_as_float(c0); v[5] = __uint_as_float(c1);
}

struct Lds {
  float red[4][128];
  float p1q[NH][128], p1g[NH][128], red3[NH][128];
  u32x4 pp[4][2][64];
  float m[4], l[4], f[4];
  float g[8];
  unsigned qs[64];
  unsigned tag, pad_[3];
  unsigned cnt[12];
};

__device__ __forceinline__ void lds_wait(unsigned* p, unsigned want) {
#pragma nounroll
  for (unsigned spin = 0; spin < SPIN_L; ++spin) {
    if (__hip_atomic_load(p, RLX_WG) >= want) break;
    __builtin_amdgcn_s_sleep(1);
  }
  CBAR();
}
constexpr float LOG2E = 1.4426950408889634f;

__device__ __forceinline__ void score32(const u32x4 (&kr)[8], u32x4 qp, bool c3, bool c2, float& s0, float& s1) {
  float d[8];
#pragma unroll
  for (int i = 0; i < 8; ++i) {
    d[i] = dot2(kr[i][0], qp[0], 0.f);
#pragma unroll
    for (int k = 1; k < 4; ++k) d[i] = dot2(kr[i][k], qp[k], d[i]);
  }
#pragma unroll
  for (int i = 0; i < 4; ++i) { const float keep = c3 ? d[i + 4] : d[i], send = c3 ? d[i] : d[i + 4]; d[i] = keep + dpp<ROR8>(send); }
#pragma unroll
  for (int i = 0; i < 2; ++i) { const float keep = c2 ? d[i + 2] : d[i], send = c2 ? d[i] : d[i + 2]; d[i] = keep + dpp<HMIR>(send); }
#pragma unroll
  for (int i = 0; i < 2; ++i) { d[i] += dpp<XOR2>(d[i]); d[i] += dpp<XOR1>(d[i]); }
  s0 = rbf(rbf(d[0]) * 0.08838834764831845f); s1 = rbf(rbf(d[1]) * 0.08838834764831845f);
}
__device__ __forceinline__ f32x4 pv32(f32x4 acc, const u32x4 (&vr)[8], unsigned pp0, unsigned pp1, u32x4 msk) {
  acc = mfma_rows(acc, vr[0], dppu<BCAST + 0>(pp0), msk);
  acc = mfma_rows(acc, vr[1], dppu<BCAST + 0>(pp1), msk);
  acc = mfma_rows(acc, vr[2], dppu<BCAST + 4>(pp0), msk);
  acc = mfma_rows(acc, vr[3], dppu<BCAST + 4>(pp1), msk);
  acc = mfma_rows(acc, vr[4], dppu<BCAST + 8>(pp0), msk);
  acc = mfma_rows(acc, vr[5], dppu<BCAST + 8>(pp1), msk);
  acc = mfma_rows(acc, vr[6], dppu<BCAST + 12>(pp0), msk);
  acc = mfma_rows(acc, vr[7], dppu<BCAST + 12>(pp1), msk);
  return acc;
}

__global__ void __launch_bounds__(NW * 64, 3) fused_kernel(const u16* __restrict__ x, const u16* __restrict__ kc, const u16* __restrict__ vc,
                                                        const u16* __restrict__ wq, const u16* __restrict__ wo, const u16* __restrict__ wg,
                                                        const u16* __restrict__ wu, const u16* __restrict__ wd, u16* __restrict__ out,
                                                        unsigned long long* __restrict__ wsp) {
  __shared__ __attribute__((aligned(16))) Lds L;
  gu64* ws = (gu64*)wsp;
  const int b = blockIdx.x, h = b >> 3, s = (b + h) & 7;
  const int tid = threadIdx.x, lane = tid & 63, w = __builtin_amdgcn_readfirstlane(tid >> 6);
  const int r = lane >> 4, c = lane & 15;
  if (tid < 12) L.cnt[tid] = 0u;
  asm volatile("" :: "s"(x), "s"(kc), "s"(vc), "s"(wq), "s"(wo), "s"(wg), "s"(wu), "s"(wd), "s"(out), "s"(wsp));
  LGKM0();
  u32x4 msk;
#pragma unroll
  for (int t = 0; t < 4; ++t) msk[t] = (c >> 1) == t ? ((c & 1) ? 0xffff0000u : 0x0000ffffu) : 0u;

  const int fj = (HQ - 1 - h) * 3 + (s - 5);
  const bool typeA = s < 5, typeB = !typeA && fj < NFI * 5;
  const int fgi = fj / 5, slice = typeA ? s : fj % 5;
  Scratch S;
  if (w >= NH) {
    const int j = (w - NH) & 3;
    const bool vwave = w >= NH + 4;
    {
      const size_t kvoff = ((size_t)h * SK + (SK - WIN) + s * KPS) * DH;
      const __amdgpu_buffer_rsrc_t rs = mkrs((vwave ? vc : kc) + kvoff, KPS * DH * 2);
      __builtin_amdgcn_s_barrier();
      const unsigned tb = (unsigned)__builtin_amdgcn_s_memrealtime() + TOFF + (vwave ? 8 * TAU : 0) + j * (TAU / 8);
#define PACE(n) do { if (((n) & 1) == 0) { _Pragma("nounroll") while ((int)((unsigned)__builtin_amdgcn_s_memrealtime() - (tb + (n) * TAU / 4)) < 0) __builtin_amdgcn_s_sleep(1); SB(); } } while (0)
#define LOADS(t, k, lo, hi) _Pragma("unroll") for (int i = lo; i < hi; ++i) { PACE(8 * k + i); t[i] = ldb(rs, lane * 16, j * 8192 + k * 32768 + i * 1024); SB(); }
      if (vwave) {
        u32x4 t0[8], t1[8], t2[8], t3[8];
#pragma nounroll
        while ((int)((unsigned)__builtin_amdgcn_s_memrealtime() - (tb - 70)) < 0) __builtin_amdgcn_s_sleep(16);
        LOADS(t0, 0, 0, 8) LOADS(t1, 1, 0, 8) LOADS(t2, 2, 0, 8) LOADS(t3, 3, 0, 4)
        lds_wait(&L.cnt[8 + j], 1u);
        const u32x4 pa = L.pp[j][0][lane], pb = L.pp[j][1][lane];
        f32x4 acc = pv32(f32x4{0.f, 0.f, 0.f, 0.f}, t0, pa[0], pa[1], msk);
        acc = pv32(acc, t1, pa[2], pa[3], msk);
        asm volatile("" : "+v"(acc));
        SB();
        LOADS(t3, 3, 4, 8)
        if (lane == 0) __hip_atomic_fetch_add(&L.cnt[3], 1u, RLX_WG);
        acc = pv32(acc, t2, pb[0], pb[1], msk);
        acc = pv32(acc, t3, pb[2], pb[3], msk);
        if (lane < 32) *(f32x4*)&L.red[j][8 * c + 4 * r] = acc;
        LGKM0();
        if (lane == 0) __hip_atomic_fetch_add(&L.cnt[4], 1u, RLX_WG);
        return;
      }
      u32x4 t0[8], t1[8], t2[8], t3[8];
      LOADS(t0, 0, 0, 8) LOADS(t1, 1, 0, 8) LOADS(t2, 2, 0, 8) LOADS(t3, 3, 0, 8)
      lds_wait(&L.cnt[2], NH);
      const u32x4 qp = *(const u32x4*)&L.qs[4 * c];
      float sc[8];
      score32(t0, qp, c & 8, c & 4, sc[0], sc[1]);
      SB();
      score32(t1, qp, c & 8, c & 4, sc[2], sc[3]);
      SB();
      score32(t2, qp, c & 8, c & 4, sc[4], sc[5]);
      score32(t3, qp, c & 8, c & 4, sc[6], sc[7]);
      float mx = fmaxf(fmaxf(fmaxf(sc[0], sc[1]), fmaxf(sc[2], sc[3])), fmaxf(fmaxf(sc[4], sc[5]), fmaxf(sc[6], sc[7])));
      mx = fmaxf(mx, dpp<HMIR>(mx)); mx = fmaxf(mx, dpp<ROR8>(mx));
      mx = rows_max(mx);
      float ls = 0.f;
      u32x4 pa, pb;
#pragma unroll
      for (int i = 0; i < 8; ++i) {
        const float e = __builtin_amdgcn_exp2f((sc[i] - mx) * LOG2E);
        ls += e;
        if (i < 4) pa[i] = pk2(e, e); else pb[i - 4] = pk2(e, e);
      }
      L.pp[j][0][lane] = pa; L.pp[j][1][lane] = pb;
      ls += dpp<HMIR>(ls); ls += dpp<ROR8>(ls);
      ls = rows_sum(ls);
      if (lane == 0) { L.m[j] = mx; L.l[j] = ls; }
      LGKM0();
      if (lane == 0) { __hip_atomic_store(&L.cnt[8 + j], 1u, RLX_WG); __hip_atomic_fetch_add(&L.cnt[6], 1u, RLX_WG); }
    }
    if (typeA || typeB) {
      const unsigned tag = __builtin_amdgcn_readfirstlane(L.tag);
      u32x4 w3[8];
      lds_wait(&L.cnt[3], 4u);
      const __amdgpu_buffer_rsrc_t rs3 = mkrs(typeA ? wo + (size_t)h * DH * D : wd + (size_t)fgi * 128 * D, (typeB && fgi == NFI - 1 ? 64 : 128) * D * 2);
#pragma unroll
      for (int i = 0; i < 8; ++i) w3[i] = ldb(rs3, (32 * j + 8 * r) * (D * 2) + slice * 256 + c * 16, i * D * 2);
      SB();
      float o0, o1;
      if (typeB) {
        const int grp = min(2 * fgi + (j >> 1), NGRP - 1);
        const __amdgpu_buffer_rsrc_t rsg = mkrs(wsp + G_GP + grp * NS * 128, NS * 1024);
        const int go = r * 1024 + (j & 1) * 256 + c * 16;
        float g[4], u[4];
        poll2s(rsg, tag, go, go + 4096, g);
        poll2s(rsg, tag, go + 512, go + 4096 + 512, u);
        const float g0 = rbf(rows_sum(g[0] + g[2])), g1 = rbf(rows_sum(g[1] + g[3])), u0 = rbf(rows_sum(u[0] + u[2])), u1 = rbf(rows_sum(u[1] + u[3]));
        o0 = rbf(1.0f / (1.0f + __expf(-g0))) * u0; o1 = rbf(1.0f / (1.0f + __expf(-g1))) * u1;
      } else {
#pragma unroll
        for (int i = 0; i < 8; ++i) asm volatile("" : "+v"(w3[i]));
        lds_wait(&L.cnt[4], 4u);
        const int gof = r * 1024 + j * 256 + c * 16;
        float v[4];
        lds_wait(&L.cnt[7], 2u);
        const float g0 = L.g[2 * r], g1 = L.g[2 * r + 1];
        poll2(S, mkrs(wsp + G_AP + h * AP_H, NS * 1024), tag, gof, gof + 4096, v);
        o0 = rows_sum(fmaf(g0, v[0], g1 * v[2])); o1 = rows_sum(fmaf(g0, v[1], g1 * v[3]));
      }
      unsigned p0 = pk2(o0, o0), p1 = pk2(o1, o1);
      p0 = __builtin_amdgcn_update_dpp(p0, p0, ROR12, 0x2, 0xf, false); p1 = __builtin_amdgcn_update_dpp(p1, p1, ROR12, 0x2, 0xf, false);
      p0 = __builtin_amdgcn_update_dpp(p0, p0, ROR8, 0x4, 0xf, false);  p1 = __builtin_amdgcn_update_dpp(p1, p1, ROR8, 0x4, 0xf, false);
      p0 = __builtin_amdgcn_update_dpp(p0, p0, ROR4, 0x8, 0xf, false);  p1 = __builtin_amdgcn_update_dpp(p1, p1, ROR4, 0x8, 0xf, false);
      f32x4 a3 = {0.f, 0.f, 0.f, 0.f}, a4 = {0.f, 0.f, 0.f, 0.f};
      a3 = mfma_rows(a3, w3[0], dppu<BCAST + 0>(p0), msk); a4 = mfma_rows(a4, w3[1], dppu<BCAST + 0>(p1), msk);
      a3 = mfma_rows(a3, w3[2], dppu<BCAST + 1>(p0), msk); a4 = mfma_rows(a4, w3[3], dppu<BCAST + 1>(p1), msk);
      a3 = mfma_rows(a3, w3[4], dppu<BCAST + 2>(p0), msk); a4 = mfma_rows(a4, w3[5], dppu<BCAST + 2>(p1), msk);
      a3 = mfma_rows(a3, w3[6], dppu<BCAST + 3>(p0), msk); a4 = mfma_rows(a4, w3[7], dppu<BCAST + 3>(p1), msk);
      if (lane < 32) *(f32x4*)&L.red3[j][8 * c + 4 * r] = a3 + a4;
      LGKM0();
      unsigned old = 0;
      if (lane == 0) old = __hip_atomic_fetch_add(&L.cnt[5], 1u, RLX_WG);
      old = __builtin_amdgcn_readfirstlane(old);
      CBAR();
      if (old == 3) {
        const float v0 = (L.red3[0][lane] + L.red3[1][lane]) + (L.red3[2][lane] + L.red3[3][lane]);
        const float v1 = (L.red3[0][lane + 64] + L.red3[1][lane + 64]) + (L.red3[2][lane + 64] + L.red3[3][lane + 64]);
        gu64* g = ws + G_FP + ((slice * 16 + (lane >> 3)) * 48 + (typeA ? h : HQ + fgi)) * 8 + (lane & 7);
        put(g, tag, v0); put(g + 8 * 48 * 8, tag, v1);
      }
      if (typeA) POLL_DRAIN(S);
    }
    return;
  }

  const bool hasg = h < NGRP;
  unsigned ep;
  asm volatile("s_load_dword %0, %1, 0x0" : "=s"(ep) : "s"(wsp) : "memory");
  {
    const __amdgpu_buffer_rsrc_t rsx = mkrs(x, D * 2);
    const int row0 = 80 * s + 20 * w + r;
    const u32x4 xa = __builtin_amdgcn_raw_buffer_load_b128(rsx, lane * 16, 0, 0);
    const u32x4 xb = __builtin_amdgcn_raw_buffer_load_b128(rsx, 1024 + lane * 16, 0, 0);
    unsigned xr[5]; u32x4 wv[5], gv[5];
#pragma unroll
    for (int i = 0; i < 5; ++i) xr[i] = __builtin_amdgcn_raw_buffer_load_b16(rsx, row0 * 2, i * 8, 0);
    const __amdgpu_buffer_rsrc_t rsw = mkrs(wq, D * HQ * DH * 2);
#pragma unroll
    for (int i = 0; i < 5; ++i) wv[i] = ldb(rsw, row0 * (HQ * DH * 2) + h * 256 + c * 16, i * 4 * (HQ * DH * 2));
    const u16* gp = (c < 8 ? wg : wu) + (hasg ? row0 * F + h * 64 + (c & 7) * 8 : 0);
    const int gstep = hasg ? 4 * F : 0;
#pragma unroll
    for (int i = 0; i < 5; ++i) gv[i] = __builtin_nontemporal_load((const u32x4*)(gp + i * gstep));
    SB();
    __builtin_amdgcn_s_barrier();
    SB();
    float sm = 0.f;
#pragma unroll
    for (int k = 0; k < 4; ++k) sm += (lo_f(xa[k]) + hi_f(xa[k])) + (lo_f(xb[k]) + hi_f(xb[k]));
    const float mean = sum64(sm) * (1.0f / D);
    float sq = 0.f, sq2 = 0.f;
#pragma unroll
    for (int k = 0; k < 4; ++k) {
      float d0 = lo_f(xa[k]) - mean, d1 = hi_f(xa[k]) - mean, d2 = lo_f(xb[k]) - mean, d3 = hi_f(xb[k]) - mean;
      sq += d0 * d0 + d1 * d1; sq2 += d2 * d2 + d3 * d3;
    }
    sq += lane < 16 ? sq2 : 0.f;
    const float var = sum64(sq) * (1.0f / D);
    const float mb = rbf(mean), den = rbf(sqrtf(rbf(rbf(var) + 1e-5f)));
    unsigned xp[5];
#pragma unroll
    for (int i = 0; i < 5; ++i) { const float xn = rbf(lo_f(xr[i]) - mb) / den; xp[i] = pk2(xn, xn); }
    f32x4 aq = {0.f, 0.f, 0.f, 0.f};
#pragma unroll
    for (int i = 0; i < 5; ++i) aq = mfma_rows(aq, wv[i], xp[i], msk);
    if (lane < 32) *(f32x4*)&L.p1q[w][8 * c + 4 * r] = aq;
    asm volatile("s_waitcnt lgkmcnt(0)" : "+s"(ep) :: "memory");
    ep = ep * 0x9E3779B1u + (unsigned)dispatch_id_intrin() * 0x85EBCA77u + 0x27D4EB2Fu;
    unsigned old = 0;
    if (lane == 0) old = __hip_atomic_fetch_add(&L.cnt[0], 1u, RLX_WG);
    old = __builtin_amdgcn_readfirstlane(old);
    CBAR();
    if (old == NH - 1) {
      const float v0 = (L.p1q[0][lane] + L.p1q[1][lane]) + (L.p1q[2][lane] + L.p1q[3][lane]);
      const float v1 = (L.p1q[0][lane + 64] + L.p1q[1][lane + 64]) + (L.p1q[2][lane + 64] + L.p1q[3][lane + 64]);
      gu64* g = ws + G_QP + (h * NS + s) * 128;
      put(g + lane, ep, v0); put(g + 64 + lane, ep, v1);
    }
    if (hasg) {
      f32x4 ag = {0.f, 0.f, 0.f, 0.f};
#pragma unroll
      for (int i = 0; i < 5; ++i) ag = mfma_rows(ag, gv[i], xp[i], msk);
      if (lane < 32) *(f32x4*)&L.p1g[w][8 * c + 4 * r] = ag;
      LGKM0();
      old = 0;
      if (lane == 0) old = __hip_atomic_fetch_add(&L.cnt[1], 1u, RLX_WG);
      old = __builtin_amdgcn_readfirstlane(old);
      CBAR();
      if (old == NH - 1) {
        const float v0 = (L.p1g[0][lane] + L.p1g[1][lane]) + (L.p1g[2][lane] + L.p1g[3][lane]);
        const float v1 = (L.p1g[0][lane + 64] + L.p1g[1][lane + 64]) + (L.p1g[2][lane + 64] + L.p1g[3][lane + 64]);
        gu64* g = ws + G_GP + (h * NS + s) * 128;
        put(g + lane, ep, v0); put(g + 64 + lane, ep, v1);
      }
    }
  }
  const unsigned tag = ep;
  const int gof = r * 1024 + w * 256 + c * 16;
  {
    __builtin_amdgcn_s_sleep(QDELAY);
    float v[4];
    poll2s(mkrs(wsp + G_QP + h * NS * 128, NS * 1024), tag, gof, gof + 4096, v);
    const float q0 = rows_sum(v[0] + v[2]), q1 = rows_sum(v[1] + v[3]);
    if (lane < 16) L.qs[16 * w + c] = pk2(q0, q1);
    if (tid == 16) L.tag = tag;
    LGKM0();
    if (lane == 0) __hip_atomic_fetch_add(&L.cnt[2], 1u, RLX_WG);
  }
  if (w == 2) {
    lds_wait(&L.cnt[6], 4u);
    const float mw = L.m[lane & 3], lw = L.l[lane & 3];
    float M = fmaxf(mw, dpp<XOR1>(mw)); M = fmaxf(M, dpp<XOR2>(M));
    const float e = __builtin_amdgcn_exp2f((mw - M) * LOG2E);
    float Ls = e * lw; Ls += dpp<XOR1>(Ls); Ls += dpp<XOR2>(Ls);
    if (lane < 2) put(ws + G_AP + h * AP_H + NS * 128 + s * 2 + lane, tag, lane ? Ls : M);
    if (lane < 4) L.f[lane] = e;
    LGKM0();
    if (lane == 0) __hip_atomic_store(&L.cnt[7], 1u, RLX_WG);
    if (typeA) {
      __builtin_amdgcn_s_sleep(QDELAY);
      float v[4];
      poll2s(mkrs_u(wsp + G_AP + h * AP_H + NS * 128, NS * 16), tag, (lane & 3) * 16, 64 + (lane & 3) * 16, v);
      float Mh = fmaxf(v[0], v[2]); Mh = fmaxf(Mh, dpp<XOR1>(Mh)); Mh = fmaxf(Mh, dpp<XOR2>(Mh));
      const float e0 = __builtin_amdgcn_exp2f((v[0] - Mh) * LOG2E), e1 = __builtin_amdgcn_exp2f((v[2] - Mh) * LOG2E);
      float Lh = fmaf(e0, v[1], e1 * v[3]);
      Lh += dpp<XOR1>(Lh); Lh += dpp<XOR2>(Lh);
      if (lane < 4) { L.g[2 * lane] = e0 / Lh; L.g[2 * lane + 1] = e1 / Lh; }
      LGKM0();
      if (lane == 0) __hip_atomic_store(&L.cnt[7], 2u, RLX_WG);
    }
  } else if (w < 2) {
    lds_wait(&L.cnt[7], 1u);
    const f32x4 f = *(const f32x4*)L.f;
    gu64* g = ws + G_AP + h * AP_H + s * 128 + tid;
    lds_wait(&L.cnt[4], 4u);
    put(g, tag, fmaf(f[0], L.red[0][tid], f[1] * L.red[1][tid]) + fmaf(f[2], L.red[2][tid], f[3] * L.red[3][tid]));
  }
  const int rj = h * 3 + (s - 5);
  if (!typeA && rj < 80 && w == 3) {
    const unsigned xw = ((const unsigned*)x)[4 * rj + (lane & 3)];
    asm volatile("" :: "v"(xw));
    lds_wait(&L.cnt[4], 4u);
    float v[6];
    poll3(S, mkrs(wsp + G_FP + rj * 48 * 8, NSRC * 64), tag, lane * 16, 1024 + lane * 16, lane < 4 * (NSRC - 32) ? 2048 + lane * 16 : OOB,
          ~((1ull << (4 * (NSRC - 32))) - 1ull), v);
    float a0 = v[0] + v[2], a1 = v[1] + v[3], b0 = v[4], b1 = v[5];
#define RED16(z) z += dpp<ROR4>(z); z += dpp<ROR8>(z); z = rows_sum(z);
    RED16(a0) RED16(a1) RED16(b0) RED16(b1)
#undef RED16
    const float r0 = rbf(lo_f(xw) + rbf(a0)) + rbf(b0), r1 = rbf(hi_f(xw) + rbf(a1)) + rbf(b1);
    if (lane < 4) ((unsigned*)out)[4 * rj + lane] = pk2(r0, r1);
    if (rj == 0 && lane == 0) __hip_atomic_store((gu32*)(ws + G_EPOCH), tag, RLX_AGENT);
    POLL_DRAIN(S);
  }
}

extern "C" void kernel_launch(void* const* d_in, const int* in_sizes, int n_in, void* d_out, int out_size, void* d_ws, size_t ws_size, hipStream_t stream) {
  fused_kernel<<<256, NW * 64, 0, stream>>>((const u16*)d_in[0], (const u16*)d_in[1], (const u16*)d_in[2], (const u16*)d_in[3], (const u16*)d_in[4],
                                            (const u16*)d_in[6], (const u16*)d_in[7], (const u16*)d_in[8], (u16*)d_out, (unsigned long long*)d_ws);
}
```
